```python
import torch

def _softmax(x, axis=-1):
    return torch.softmax(x, dim=axis)
def _gelu(x):
    return torch.nn.functional.gelu(x, approximate="tanh")
def _silu(x):
    return torch.nn.functional.silu(x)
def _sigmoid(x):
    return torch.sigmoid(x)
def _layernorm(x, axis=-1, eps=1e-5):
    m = x.mean(dim=axis, keepdim=True)
    v = x.var(dim=axis, keepdim=True, unbiased=False)
    return (x - m) / torch.sqrt(v + eps)
def _scatter_add(tgt, idx, src, axis=0):
    return tgt.clone().index_add_(axis, idx, src)
def _segment_sum(src, seg, n):
    out = torch.zeros((n,) + src.shape[1:], dtype=src.dtype)
    return out.index_add_(0, seg, src)
def _pad(x, pw):
    flat = tuple(v for pair in reversed(pw) for v in pair)
    return torch.nn.functional.pad(x, flat)

kernel_name = 'standalone__gqa_softmax__near_ridge'

def setup_inputs():

    _w = (lambda a: (lambda t: t.to(torch.bfloat16) if t.is_floating_point() else t)(torch.as_tensor(a)))
    B, Hq, Hkv, Sq, Sk, D = 32, 32, 4, 192, 512, 128
    return {
        "q": _w((lambda *s: torch.randn(*s, dtype=torch.bfloat16) * 0.8)(B, Hq, Sq, D)),
        "k": _w((lambda *s: torch.randn(*s, dtype=torch.bfloat16) * 0.8)(B, Hkv, Sk, D)),
        "v": _w((lambda *s: torch.randn(*s, dtype=torch.bfloat16) * 0.05)(B, Hkv, Sk, D)),
    }

def reference(q, k, v):
    B, Hq, Sq, D = q.shape
    Hkv = k.shape[1]
    rep = Hq // Hkv
    k = (lambda x, n, axis: torch.repeat_interleave(x, n, dim=axis))(k, rep, axis=1)
    v = (lambda x, n, axis: torch.repeat_interleave(x, n, dim=axis))(v, rep, axis=1)
    scale = D ** -0.5
    scores = torch.einsum("bhqd,bhkd->bhqk", q, k) * scale
    p = _softmax(scores, axis=-1)
    return torch.einsum("bhqk,bhkd->bhqd", p, v)
```

```cpp
#include <hip/hip_runtime.h>
#include <hip/hip_bf16.h>
#include <cstdio>
using bf16 = __hip_bfloat16;
constexpr int   D = 128, NW = 8, QBLK = 32, KVBLK = 64;
constexpr float SCALE = 0.088388347648318440f;
constexpr float THR = 8.f;
constexpr int SDEPTH = 2;
constexpr int LDQ = D, LDK = D, LDO = D;
constexpr size_t SHM_V = KVBLK * D * 2, SHM_K = KVBLK * D * 2, SHM_ATTN = 2 * SHM_V + 2 * SHM_K + NW * 64 * 4;
using bf16x8 = __attribute__((ext_vector_type(8))) short;
using s16x4  = __attribute__((ext_vector_type(4))) short;
using f32x16 = __attribute__((ext_vector_type(16))) float;
using f32x8  = __attribute__((ext_vector_type(8))) float;
using u32x4  = __attribute__((ext_vector_type(4))) unsigned;
#define KSWZ(row, colB) ((row) * 256 + ((colB) ^ (((row) & 7) << 4)))
#define SBAR() __builtin_amdgcn_sched_barrier(0)
__device__ __forceinline__ int crow(int r, int hi) { return (r & 3) + 8 * (r >> 2) + 4 * hi; }
__device__ __forceinline__ unsigned cvtpk(float lo, float hi) {
  unsigned r; asm volatile("v_cvt_pk_bf16_f32 %0, %1, %2" : "=v"(r) : "v"(lo), "v"(hi)); return r;
}
__device__ __forceinline__ bf16x8 ld8(const bf16* p) { return *reinterpret_cast<const bf16x8*>(p); }
__device__ __forceinline__ void partialSM(f32x16& p0, f32x16& p1, float& m_reg, float& mn, float& alpha) {
  constexpr float C = SCALE * 1.4426950408889634f;
  float pmax = p0[0]; for (int r = 1; r < 16; ++r) pmax = fmaxf(pmax, p0[r]); for (int r = 0; r < 16; ++r) pmax = fmaxf(pmax, p1[r]);
  { auto rr = __builtin_amdgcn_permlane32_swap(__float_as_uint(pmax), __float_as_uint(pmax), false, false);
    pmax = fmaxf(__uint_as_float(rr[0]), __uint_as_float(rr[1])); }
  if (__builtin_expect(__all(pmax - m_reg <= THR / SCALE), 1)) { mn = m_reg; alpha = 1.f; }
  else { mn = fmaxf(m_reg, pmax); alpha = __builtin_amdgcn_exp2f((m_reg - mn) * C); m_reg = mn; }
  float mnC = -mn * C;
  for (int r = 0; r < 16; ++r) p0[r] = fmaf(p0[r], C, mnC); for (int r = 0; r < 16; ++r) p1[r] = fmaf(p1[r], C, mnC);
  for (int r = 0; r < 16; ++r) p0[r] = __builtin_amdgcn_exp2f(p0[r]);
}
__device__ __forceinline__ void finishSM(f32x16& p0, f32x16& p1, float alpha, float& l_reg, bf16x8& pa0, bf16x8& pa1, bf16x8& pa2, bf16x8& pa3) {
  for (int r = 0; r < 16; ++r) p1[r] = __builtin_amdgcn_exp2f(p1[r]);
  float ps = 0; for (int r = 0; r < 16; ++r) ps += p0[r]; for (int r = 0; r < 16; ++r) ps += p1[r];
  { auto rr = __builtin_amdgcn_permlane32_swap(__float_as_uint(ps), __float_as_uint(ps), false, false);
    ps = __uint_as_float(rr[0]) + __uint_as_float(rr[1]); }
  l_reg = l_reg * alpha + ps;
#define PK4(P, BASE, OUT) do { unsigned a0 = cvtpk(P[BASE + 0], P[BASE + 1]), a1 = cvtpk(P[BASE + 2], P[BASE + 3]);   \
    unsigned b0 = cvtpk(P[BASE + 4], P[BASE + 5]), b1 = cvtpk(P[BASE + 6], P[BASE + 7]);                              \
    auto r0 = __builtin_amdgcn_permlane32_swap(a0, b0, false, false); auto r1 = __builtin_amdgcn_permlane32_swap(a1, b1, false, false); \
    u32x4 w = {r0[0], r1[0], r0[1], r1[1]}; OUT = *reinterpret_cast<bf16x8*>(&w); } while (0)
  PK4(p0, 0, pa0); PK4(p0, 8, pa1); PK4(p1, 0, pa2); PK4(p1, 8, pa3);
#undef PK4
}
__device__ __forceinline__ void qkt(f32x16& p0, f32x16& p1, const bf16* Ks, const bf16x8* qr, int r32, int hi) {
  p0 = f32x16{}; p1 = f32x16{};
  for (int d0 = 0; d0 < 8; ++d0) { int cb = (d0 * 16 + hi * 8) * 2;
    bf16x8 b0 = *reinterpret_cast<const bf16x8*>((const char*)Ks + KSWZ(r32, cb));
    bf16x8 b1 = *reinterpret_cast<const bf16x8*>((const char*)Ks + KSWZ(32 + r32, cb));
    p0 = __builtin_amdgcn_mfma_f32_32x32x16_bf16(b0, qr[d0], p0, 0, 0, 0);
    p1 = __builtin_amdgcn_mfma_f32_32x32x16_bf16(b1, qr[d0], p1, 0, 0, 0); }
}
__device__ __forceinline__ int v_st(int k, int c) { const int kk = (k & ~0xC) | ((k & 4) << 1) | ((k & 8) >> 1); return ((kk >> 3) * 4 + (c >> 5)) * 512 + ((kk & 7) * 32 + (c & 31)) * 2; }
__device__ __forceinline__ int v_rd_base(int lane) { return ((lane & 3) << 3) | (((lane >> 2) & 3) << 6) | (((lane >> 4) & 1) << 5) | (((lane >> 5) & 1) << 8); }
constexpr int v_rd_off(int d0, int ks, int half) { return d0 * 512 + ks * 4096 + half * 2048; }
template <int OFF> __device__ __forceinline__ s16x4 tr_read(int vb) {
  s16x4 r; asm volatile("ds_read_b64_tr_b16 %0, %1 offset:%2" : "=&v"(r) : "v"(vb), "i"(OFF) : "memory"); return r;
}
template <int D0> __device__ __forceinline__ void pv_one(f32x16& od, int vb, bf16x8 pa0, bf16x8 pa1, bf16x8 pa2, bf16x8 pa3) {
  const s16x4 l0 = tr_read<v_rd_off(D0, 0, 0)>(vb), h0 = tr_read<v_rd_off(D0, 0, 1)>(vb), l1 = tr_read<v_rd_off(D0, 1, 0)>(vb), h1 = tr_read<v_rd_off(D0, 1, 1)>(vb);
  const s16x4 l2 = tr_read<v_rd_off(D0, 2, 0)>(vb), h2 = tr_read<v_rd_off(D0, 2, 1)>(vb), l3 = tr_read<v_rd_off(D0, 3, 0)>(vb), h3 = tr_read<v_rd_off(D0, 3, 1)>(vb);
  asm volatile("s_waitcnt lgkmcnt(0)" ::: "memory"); SBAR();
#define PK(L, H) (bf16x8){L[0], L[1], L[2], L[3], H[0], H[1], H[2], H[3]}
  od = __builtin_amdgcn_mfma_f32_32x32x16_bf16(pa0, PK(l0, h0), od, 0, 0, 0);
  od = __builtin_amdgcn_mfma_f32_32x32x16_bf16(pa1, PK(l1, h1), od, 0, 0, 0);
  od = __builtin_amdgcn_mfma_f32_32x32x16_bf16(pa2, PK(l2, h2), od, 0, 0, 0);
  od = __builtin_amdgcn_mfma_f32_32x32x16_bf16(pa3, PK(l3, h3), od, 0, 0, 0);
#undef PK
}
__device__ __forceinline__ void pv_d0(f32x16* o, int vb, bf16x8 pa0, bf16x8 pa1, bf16x8 pa2, bf16x8 pa3) {
  pv_one<0>(o[0], vb, pa0, pa1, pa2, pa3); pv_one<1>(o[1], vb, pa0, pa1, pa2, pa3); pv_one<2>(o[2], vb, pa0, pa1, pa2, pa3); pv_one<3>(o[3], vb, pa0, pa1, pa2, pa3);
}
__device__ __forceinline__ void attn_dense_body(const bf16* __restrict__ Qb, const bf16* __restrict__ Kh, const bf16* __restrict__ Vh,
                                                bf16* __restrict__ Ob, int seq, char* lds) {
  const int tid = threadIdx.x, wid = tid >> 6, lane = tid & 63, r32 = lane & 31, hi = lane >> 5;
  bf16* V_lds = (bf16*)lds; bf16* K_lds = (bf16*)(lds + 2 * SHM_V);
  float* ws = (float*)(lds + 2 * SHM_V + 2 * SHM_K) + wid * 64; float* li_l = ws; float* al_l = ws + 32;
  float m_reg = -1e30f, l_reg = 0; f32x16 o[4] = {}; bf16x8 qr[8];
  const bf16* Qw = Qb + (long)(wid * QBLK + r32) * LDQ + hi * 8;
#pragma unroll
  for (int d0 = 0; d0 < 8; ++d0) qr[d0] = ld8(Qw + d0 * 16);
  const int sr = tid >> 4, sc = (tid & 15) * 8, vst0 = v_st(sr, sc), vst1 = v_st(32 + sr, sc);
  const int vb0 = (int)(uintptr_t)V_lds + v_rd_base(lane);
  struct { bf16x8 vs0, vs1, ks0, ks1; } sr_[SDEPTH];
#define SLOAD(i, k0) do { sr_[i].vs0 = ld8(&Vh[(long)((k0) + sr) * LDK + sc]); sr_[i].vs1 = ld8(&Vh[(long)((k0) + 32 + sr) * LDK + sc]); \
    sr_[i].ks0 = ld8(&Kh[(long)((k0) + sr) * LDK + sc]); sr_[i].ks1 = ld8(&Kh[(long)((k0) + 32 + sr) * LDK + sc]); } while (0)
#define SWRITE(b, i) do { *(bf16x8*)((char*)V_lds + (b) * SHM_V + vst0) = (sr_[i].vs0);          \
    *(bf16x8*)((char*)V_lds + (b) * SHM_V + vst1) = (sr_[i].vs1); int kc = sc * 2;               \
    *(bf16x8*)((char*)K_lds + (b) * SHM_K + KSWZ(sr, kc)) = (sr_[i].ks0);                       \
    *(bf16x8*)((char*)K_lds + (b) * SHM_K + KSWZ(32 + sr, kc)) = (sr_[i].ks1); } while (0)
#define SWAIT() do { asm volatile("s_waitcnt vmcnt(4)" ::: "memory"); } while (0)
#define RESC(a) do { if (__any((a) < 1.f)) { if (hi == 0) al_l[r32] = (a); asm volatile("s_waitcnt lgkmcnt(0)" ::: "memory"); \
    for (int d = 0; d < 4; ++d) for (int r = 0; r < 16; ++r) o[d][r] *= al_l[crow(r, hi)]; } } while (0)
  f32x16 pA0, pA1, pB0, pB1; float mnA, mnB, alA, alB; bf16x8 pa0, pa1, pa2, pa3; const int NT = seq / KVBLK;
  constexpr int SE = 0, SO = SDEPTH - 1;
  SLOAD(SE, 0); asm volatile("s_waitcnt vmcnt(0)" ::: "memory"); SWRITE(0, SE); __syncthreads();
  qkt(pA0, pA1, K_lds, qr, r32, hi); partialSM(pA0, pA1, m_reg, mnA, alA);
  SLOAD(SO, KVBLK); { if (2 < NT) SLOAD(SE, 2 * KVBLK); }
  SWAIT(); SWRITE(1, SO); __syncthreads();
  for (int j = 1; j + 1 < NT; j += 2) {
    SBAR(); qkt(pB0, pB1, (bf16*)((char*)K_lds + SHM_K), qr, r32, hi);
    finishSM(pA0, pA1, alA, l_reg, pa0, pa1, pa2, pa3); SBAR();
    SLOAD(SO, (j + SDEPTH) * KVBLK); SBAR();
    pv_d0(o, vb0, pa0, pa1, pa2, pa3); partialSM(pB0, pB1, m_reg, mnB, alB);
    __syncthreads(); SWAIT(); SWRITE(0, SE);
    RESC(alB); __syncthreads();
    SBAR(); qkt(pA0, pA1, K_lds, qr, r32, hi);
    finishSM(pB0, pB1, alB, l_reg, pa0, pa1, pa2, pa3); SBAR();
    if (j + 3 < NT) SLOAD(SE, (j + 1 + SDEPTH) * KVBLK); SBAR();
    pv_d0(o, vb0 + (int)SHM_V, pa0, pa1, pa2, pa3); partialSM(pA0, pA1, m_reg, mnA, alA);
    __syncthreads(); SWAIT(); SWRITE(1, SO);
    RESC(alA); __syncthreads();
  }
  SBAR(); qkt(pB0, pB1, (bf16*)((char*)K_lds + SHM_K), qr, r32, hi);
  finishSM(pA0, pA1, alA, l_reg, pa0, pa1, pa2, pa3); SBAR();
  pv_d0(o, vb0, pa0, pa1, pa2, pa3); partialSM(pB0, pB1, m_reg, mnB, alB);
  __syncthreads(); RESC(alB);
  finishSM(pB0, pB1, alB, l_reg, pa0, pa1, pa2, pa3); SBAR();
  pv_d0(o, vb0 + (int)SHM_V, pa0, pa1, pa2, pa3);
  if (hi == 0) li_l[r32] = l_reg; asm volatile("s_waitcnt lgkmcnt(0)" ::: "memory");
  float rli[16];
#pragma unroll
  for (int r = 0; r < 16; ++r) rli[r] = __builtin_amdgcn_rcpf(li_l[crow(r, hi)]);
  bf16* Ow = Ob + (long)(wid * QBLK) * LDO;
#pragma unroll
  for (int r = 0; r < 16; ++r) { int orow = crow(r, hi);
    for (int d0 = 0; d0 < 4; ++d0) Ow[(long)orow * LDO + d0 * 32 + r32] = __float2bfloat16(o[d0][r] * rli[r]); }
}
__global__ __launch_bounds__(64 * NW, 1)
void attn_fwd_dense(const bf16* __restrict__ Q, const bf16* __restrict__ K, const bf16* __restrict__ V, bf16* __restrict__ O) {
  extern __shared__ __attribute__((aligned(16))) char shm[];
  const int bid = blockIdx.x;
  const int xcd = bid & 7, idx = bid >> 3;
  const int p = xcd * 16 + idx / 6, c = idx % 6;
  const long q0 = ((long)p * 1536 + c * 256) * D, k0 = (long)p * 512 * D;
  attn_dense_body(Q + q0, K + k0, V + k0, O + q0, 512, shm);
}
extern "C" void kernel_launch(void* const* d_in, const int* in_sizes, int n_in, void* d_out, int out_size, void* d_ws, size_t ws_size, hipStream_t stream) {
  hipFuncSetAttribute((const void*)attn_fwd_dense, hipFuncAttributeMaxDynamicSharedMemorySize, (int)SHM_ATTN);
  hipLaunchKernelGGL(attn_fwd_dense, dim3(768), dim3(64 * NW), SHM_ATTN, stream,
                     (const bf16*)d_in[0], (const bf16*)d_in[1], (const bf16*)d_in[2], (bf16*)d_out);
}
```

```cpp
#include <hip/hip_runtime.h>
#include <hip/hip_bf16.h>

#define STAMP 0

typedef unsigned u32x4 __attribute__((ext_vector_type(4)));

__device__ __forceinline__ float bf_lo(unsigned w) { return __uint_as_float(w << 16); }
__device__ __forceinline__ float bf_hi(unsigned w) { return __uint_as_float(w & 0xffff0000u); }

__device__ __forceinline__ void slow_rows(const char* __restrict__ Qb, const char* __restrict__ Kb, const char* __restrict__ Vb,
                                          char* __restrict__ Ob, unsigned wv) {
  unsigned lane;
  asm volatile("v_mbcnt_lo_u32_b32 %0, -1, 0\n\tv_mbcnt_hi_u32_b32 %0, -1, %0" : "=v"(lane));
  const float scale = 0.08838834764831845f;
#pragma unroll 1
  for (int rr = 0; rr < 192; ++rr) {
    const unsigned row = (unsigned)(rr >> 6) * 256u + wv * 64u + (unsigned)(rr & 63);
    const u32x4* q = (const u32x4*)(Qb + (size_t)row * 256);
    float s[8];
#pragma unroll
    for (int kk = 0; kk < 8; ++kk) {
      const u32x4* k = (const u32x4*)(Kb + (size_t)(kk * 64 + lane) * 256);
      float acc = 0.f;
#pragma unroll 1
      for (int c = 0; c < 16; ++c) {
        const u32x4 a = q[c], b = k[c];
#pragma unroll
        for (int e = 0; e < 4; ++e) { acc = fmaf(bf_lo(a[e]), bf_lo(b[e]), acc); acc = fmaf(bf_hi(a[e]), bf_hi(b[e]), acc); }
      }
      s[kk] = acc * scale;
    }
    float m = s[0];
#pragma unroll
    for (int kk = 1; kk < 8; ++kk) m = fmaxf(m, s[kk]);
#pragma unroll
    for (int o = 32; o > 0; o >>= 1) m = fmaxf(m, __shfl_xor(m, o));
    float l = 0.f;
#pragma unroll
    for (int kk = 0; kk < 8; ++kk) { s[kk] = __expf(s[kk] - m); l += s[kk]; }
#pragma unroll
    for (int o = 32; o > 0; o >>= 1) l += __shfl_xor(l, o);
    float o0 = 0.f, o1 = 0.f;
#pragma unroll
    for (int kk = 0; kk < 8; ++kk) {
#pragma unroll 1
      for (int src = 0; src < 64; ++src) {
        const float pk = __shfl(s[kk], src);
        const unsigned w = *(const unsigned*)(Vb + (size_t)(kk * 64 + src) * 256 + lane * 4);
        o0 = fmaf(pk, bf_lo(w), o0); o1 = fmaf(pk, bf_hi(w), o1);
      }
    }
    const float il = 1.f / l;
    const __hip_bfloat16 b0 = __float2bfloat16(o0 * il), b1 = __float2bfloat16(o1 * il);
    const unsigned w = (unsigned)__builtin_bit_cast(unsigned short, b0) | ((unsigned)__builtin_bit_cast(unsigned short, b1) << 16);
    *(unsigned*)(Ob + (size_t)row * 256 + lane * 4) = w;
  }
}

extern "C" __global__ __launch_bounds__(256, 1)
void gqa_fwd(const char* __restrict__ Q, const char* __restrict__ K, const char* __restrict__ V, char* __restrict__ O
             ) {
  asm volatile("s_getpc_b64 s[56:57]\n.LCODE0:\n\ts_and_b32 s57, s57, 0xffff\n\ts_mov_b32 s58, .LCODEEND-.LCODE0\n\ts_mov_b32 s59, 0x20000\n\t"
               "s_lshr_b32 s60, %0, 3\n\ts_lshl_b32 s60, s60, 11\n\tv_lshrrev_b32 v207, 4, %1\n\tv_lshlrev_b32 v207, 7, v207\n\t"
               "buffer_load_dword a245, v207, s[56:59], s60 offen"
               :: "s"(blockIdx.x), "v"(threadIdx.x) : "s56", "s57", "s58", "s59", "s60", "v207", "a245", "scc");
  const unsigned bid = blockIdx.x;
  const unsigned pair = (bid & 7u) + 8u * (bid >> 4), half = (bid >> 3) & 1u;
  const unsigned wv = __builtin_amdgcn_readfirstlane(threadIdx.x >> 6);
  const char* Qb = Q + ((size_t)pair * 1536 + half * 768) * 256;
  char* Ob = O + ((size_t)pair * 1536 + half * 768) * 256;
  const char* Kb = K + (size_t)pair * 131072;
  const char* Vb = V + (size_t)pair * 131072;
  const unsigned long long qa = (unsigned long long)Qb, ka = (unsigned long long)Kb, va = (unsigned long long)Vb, oa = (unsigned long long)Ob;
  const unsigned grp = (bid >> 4) & 3u;
  unsigned bad;
  asm volatile(R"ASM(
.macro QZ d,a,b
v_mfma_f32_16x16x32_bf16 v[\d:\d+3], a[\a:\a+3], a[\b:\b+3], 0
.endm
.macro QK d,a,b
v_mfma_f32_16x16x32_bf16 v[\d:\d+3], a[\a:\a+3], a[\b:\b+3], v[\d:\d+3]
.endm
.macro PZ d,a,b
v_mfma_f32_16x16x32_bf16 v[\d:\d+3], a[\a:\a+3], v[\b:\b+3], 0
.endm
.macro PV d,a,b
v_mfma_f32_16x16x32_bf16 v[\d:\d+3], a[\a:\a+3], v[\b:\b+3], v[\d:\d+3]
.endm
.macro SZ d,b
v_mfma_f32_16x16x32_bf16 a[\d:\d+3], a[240:243], v[\b:\b+3], 0
.endm
.macro SM d,b
v_mfma_f32_16x16x32_bf16 a[\d:\d+3], a[240:243], v[\b:\b+3], a[\d:\d+3]
.endm
.macro SF d,b
v_smfmac_f32_16x16x64_bf16 a[\d:\d+3], a[240:243], v[\b:\b+7], v252
.endm
.macro E r
v_exp_f32 v\r, v\r
.endm
.macro C d,a
v_cvt_pk_bf16_f32 v\d, v\a, v[\a+1]
.endm
.macro KR a,b,o
ds_read_b128 a[\a:\a+3], v\b offset:\o
.endm
.macro VR a,b,o
ds_read_b64_tr_b16 a[\a:\a+1], v\b offset:\o
.endm
.macro MU r,s
v_mul_f32 v\r, v\r, v\s
.endm
v_mbcnt_lo_u32_b32 v200, -1, 0
v_mbcnt_hi_u32_b32 v200, -1, v200
s_mov_b32 s68, %[klo]
s_and_b32 s69, %[khi], 0xffff
s_mov_b32 s70, 0x20000
s_mov_b32 s71, 0x20000
s_lshl_b32 s80, %[wv], 12
s_lshl_b32 s81, %[wv], 14
v_lshlrev_b32 v255, 4, v200
s_mov_b32 s64, %[qlo]
s_and_b32 s65, %[qhi], 0xffff
s_mov_b32 s66, 0x30000
s_mov_b32 s67, 0x20000
v_lshrrev_b32 v201, 4, v200
v_and_b32 v202, 15, v200
v_lshlrev_b32 v246, 8, v202
v_lshl_add_u32 v246, v201, 4, v246
s_mov_b32 s85, s80
buffer_load_dwordx4 v[128:131], v255, s[68:71], s85 offen offset:0
buffer_load_dwordx4 v[132:135], v255, s[68:71], s85 offen offset:1024
buffer_load_dwordx4 v[136:139], v255, s[68:71], s85 offen offset:2048
buffer_load_dwordx4 v[140:143], v255, s[68:71], s85 offen offset:3072
s_add_u32 s86, s81, 0
buffer_load_dwordx4 a[128:131], v246, s[64:67], s86 offen offset:0
buffer_load_dwordx4 a[132:135], v246, s[64:67], s86 offen offset:64
buffer_load_dwordx4 a[136:139], v246, s[64:67], s86 offen offset:128
buffer_load_dwordx4 a[140:143], v246, s[64:67], s86 offen offset:192
s_add_u32 s86, s81, 4096
buffer_load_dwordx4 a[144:147], v246, s[64:67], s86 offen offset:0
buffer_load_dwordx4 a[148:151], v246, s[64:67], s86 offen offset:64
buffer_load_dwordx4 a[152:155], v246, s[64:67], s86 offen offset:128
buffer_load_dwordx4 a[156:159], v246, s[64:67], s86 offen offset:192
s_mov_b32 s72, %[vlo]
s_and_b32 s73, %[vhi], 0xffff
s_mov_b32 s74, 0x20000
s_mov_b32 s75, 0x20000
v_bfe_u32 v203, v200, 2, 2
v_lshlrev_b32 v203, 8, v203
v_lshl_add_u32 v203, v201, 6, v203
v_and_b32 v204, 3, v200
v_lshl_add_u32 v244, v204, 4, v203
v_xor_b32 v245, 32, v244
s_mov_b32 s76, %[olo]
s_and_b32 s77, %[ohi], 0xffff
s_mov_b32 s78, 0x30000
s_mov_b32 s79, 0x20000
s_mov_b32 s82, 0x3e0293ee
s_mov_b32 s83, 0xffff0000
s_getpc_b64 s[56:57]
.LPC1:
s_sub_u32 s56, s56, .LPC1-.LCODE0
s_subb_u32 s57, s57, 0
s_and_b32 s57, s57, 0xffff
s_mov_b32 s58, .LCODEEND-.LCODE0
s_mov_b32 s59, 0x20000
v_lshrrev_b32 v252, 3, v200
v_lshlrev_b32 v252, 7, v252
s_lshl_b32 s60, %[wv], 10
v_add_u32 v252, s60, v252
s_mov_b32 s88, 0x21800000
v_lshrrev_b32 v203, 2, v202
v_lshlrev_b32 v204, 10, v203
v_lshl_add_u32 v204, v201, 6, v204
v_add_u32 v204, s80, v204
v_and_b32 v205, 3, v202
v_xor_b32 v184, 0, v205
v_lshl_add_u32 v184, v184, 4, v204
v_xor_b32 v185, 3, v205
v_lshl_add_u32 v185, v185, 4, v204
v_xor_b32 v186, 2, v205
v_lshl_add_u32 v186, v186, 4, v204
v_xor_b32 v187, 1, v205
v_lshl_add_u32 v187, v187, 4, v204
v_sub_u32 v204, 0, v203
v_and_b32 v204, 3, v204
v_xor_b32 v204, v201, v204
v_lshlrev_b32 v204, 4, v204
v_lshl_add_u32 v240, v202, 6, v204
v_add_u32 v241, 0x10000, v240
v_lshlrev_b32 v204, 10, v201
v_lshl_add_u32 v204, v203, 6, v204
v_lshl_add_u32 v204, v205, 3, v204
v_and_b32 v205, 1, v201
v_lshl_add_u32 v204, v205, 5, v204
v_add_u32 v242, 131072, v204
v_xor_b32 v243, 32, v242
v_add_u32 v188, 0x10000, v204
v_xor_b32 v189, 32, v188
v_lshlrev_b32 v205, 1, v205
v_lshrrev_b32 v204, 1, v201
v_or_b32 v204, v204, v205
v_lshlrev_b32 v204, 4, v204
v_lshl_add_u32 v247, v202, 8, v204
v_and_b32 v204, 3, v200
v_cmp_eq_u32 vcc, 0, v204
v_mov_b32 v205, 0x3f803f80
v_mov_b32 v254, 0
s_nop 1
v_cndmask_b32 v204, 0, v205, vcc
v_accvgpr_write_b32 a240, v204
v_accvgpr_write_b32 a241, v204
v_accvgpr_write_b32 a242, v204
v_accvgpr_write_b32 a243, v204
s_add_u32 s84, s80, 0
s_add_u32 m0, s80, 131072
s_add_u32 s85, s80, 16384
s_waitcnt vmcnt(11)
buffer_load_dwordx4 v244, s[72:75], s84 offen offset:0  lds
v_lshlrev_b32 v176, 16, v128
v_lshlrev_b32 v177, 16, v129
v_lshlrev_b32 v178, 16, v130
v_lshlrev_b32 v179, 16, v131
v_and_b32 v128, s83, v128
v_and_b32 v129, s83, v129
v_and_b32 v130, s83, v130
v_and_b32 v131, s83, v131
v_mul_f32 v176, s82, v176
v_mul_f32 v177, s82, v177
v_mul_f32 v178, s82, v178
v_mul_f32 v179, s82, v179
v_mul_f32 v128, s82, v128
v_mul_f32 v129, s82, v129
v_mul_f32 v130, s82, v130
v_mul_f32 v131, s82, v131
v_cvt_pk_bf16_f32 v128, v176, v128
v_cvt_pk_bf16_f32 v129, v177, v129
v_cvt_pk_bf16_f32 v130, v178, v130
v_cvt_pk_bf16_f32 v131, v179, v131
ds_write_b128 v184, v[128:131] offset:0
s_waitcnt vmcnt(11)
buffer_load_dwordx4 v245, s[72:75], s84 offen offset:1024  lds
v_lshlrev_b32 v180, 16, v132
v_lshlrev_b32 v181, 16, v133
v_lshlrev_b32 v182, 16, v134
v_lshlrev_b32 v183, 16, v135
v_and_b32 v132, s83, v132
v_and_b32 v133, s83, v133
v_and_b32 v134, s83, v134
v_and_b32 v135, s83, v135
v_mul_f32 v180, s82, v180
v_mul_f32 v181, s82, v181
v_mul_f32 v182, s82, v182
v_mul_f32 v183, s82, v183
v_mul_f32 v132, s82, v132
v_mul_f32 v133, s82, v133
v_mul_f32 v134, s82, v134
v_mul_f32 v135, s82, v135
v_cvt_pk_bf16_f32 v132, v180, v132
v_cvt_pk_bf16_f32 v133, v181, v133
v_cvt_pk_bf16_f32 v134, v182, v134
v_cvt_pk_bf16_f32 v135, v183, v135
ds_write_b128 v185, v[132:135] offset:256
s_waitcnt vmcnt(11)
buffer_load_dwordx4 v244, s[72:75], s84 offen offset:2048  lds
v_lshlrev_b32 v176, 16, v136
v_lshlrev_b32 v177, 16, v137
v_lshlrev_b32 v178, 16, v138
v_lshlrev_b32 v179, 16, v139
v_and_b32 v136, s83, v136
v_and_b32 v137, s83, v137
v_and_b32 v138, s83, v138
v_and_b32 v139, s83, v139
v_mul_f32 v176, s82, v176
v_mul_f32 v177, s82, v177
v_mul_f32 v178, s82, v178
v_mul_f32 v179, s82, v179
v_mul_f32 v136, s82, v136
v_mul_f32 v137, s82, v137
v_mul_f32 v138, s82, v138
v_mul_f32 v139, s82, v139
v_cvt_pk_bf16_f32 v136, v176, v136
v_cvt_pk_bf16_f32 v137, v177, v137
v_cvt_pk_bf16_f32 v138, v178, v138
v_cvt_pk_bf16_f32 v139, v179, v139
ds_write_b128 v186, v[136:139] offset:512
s_waitcnt vmcnt(11)
buffer_load_dwordx4 v245, s[72:75], s84 offen offset:3072  lds
v_lshlrev_b32 v180, 16, v140
v_lshlrev_b32 v181, 16, v141
v_lshlrev_b32 v182, 16, v142
v_lshlrev_b32 v183, 16, v143
v_and_b32 v140, s83, v140
v_and_b32 v141, s83, v141
v_and_b32 v142, s83, v142
v_and_b32 v143, s83, v143
v_mul_f32 v180, s82, v180
v_mul_f32 v181, s82, v181
v_mul_f32 v182, s82, v182
v_mul_f32 v183, s82, v183
v_mul_f32 v140, s82, v140
v_mul_f32 v141, s82, v141
v_mul_f32 v142, s82, v142
v_mul_f32 v143, s82, v143
v_cvt_pk_bf16_f32 v140, v180, v140
v_cvt_pk_bf16_f32 v141, v181, v141
v_cvt_pk_bf16_f32 v142, v182, v142
v_cvt_pk_bf16_f32 v143, v183, v143
ds_write_b128 v187, v[140:143] offset:768
s_waitcnt lgkmcnt(0)
s_barrier
KR 0,240,0
KR 4,240,1024
KR 8,240,2048
KR 12,240,3072
KR 16,240,4096
KR 20,240,5120
KR 24,240,6144
KR 28,240,7168
KR 32,240,8192
KR 36,240,9216
KR 40,240,10240
KR 44,240,11264
KR 48,240,12288
KR 52,240,13312
KR 56,240,14336
KR 60,240,15360
s_waitcnt vmcnt(8)
buffer_load_dwordx4 v[144:147], v255, s[68:71], s85 offen offset:0
buffer_load_dwordx4 v[148:151], v255, s[68:71], s85 offen offset:1024
buffer_load_dwordx4 v[152:155], v255, s[68:71], s85 offen offset:2048
buffer_load_dwordx4 v[156:159], v255, s[68:71], s85 offen offset:3072
s_waitcnt lgkmcnt(12)
QZ 192,0,128
s_add_u32 s86, s81, 8192
QK 192,4,132
QK 192,8,136
QK 192,12,140
s_waitcnt lgkmcnt(8)
s_waitcnt vmcnt(8)
QZ 196,16,128
buffer_load_dwordx4 a[160:163], v246, s[64:67], s86 offen offset:0
QK 196,20,132
QK 196,24,136
buffer_load_dwordx4 a[164:167], v246, s[64:67], s86 offen offset:64
QK 196,28,140
s_waitcnt lgkmcnt(4)
QZ 200,32,128
E 192
QK 200,36,132
buffer_load_dwordx4 a[168:171], v246, s[64:67], s86 offen offset:128
QK 200,40,136
E 194
QK 200,44,140
s_waitcnt lgkmcnt(0)
QZ 204,48,128
buffer_load_dwordx4 a[172:175], v246, s[64:67], s86 offen offset:192
QK 204,52,132
E 193
QK 204,56,136
s_waitcnt vmcnt(8)
s_barrier
QK 204,60,140
VR 64,242,0
s_add_u32 s86, s81, 12288
.LSTEP0:
QZ 208,0,144
buffer_load_dwordx4 a[176:179], v246, s[64:67], s86 offen offset:0
QK 208,4,148
VR 66,242,4096
C 224,192
QK 208,8,152
VR 96,242,8192
VR 98,242,12288
QK 208,12,156
VR 68,243,0
QZ 212,16,144
buffer_load_dwordx4 a[180:183], v246, s[64:67], s86 offen offset:64
QK 212,20,148
VR 70,243,4096
VR 100,243,8192
QK 212,24,152
VR 102,243,12288
QK 212,28,156
VR 72,242,256
QZ 216,32,144
buffer_load_dwordx4 a[184:187], v246, s[64:67], s86 offen offset:128
QK 216,36,148
VR 74,242,4352
VR 104,242,8448
QK 216,40,152
VR 106,242,12544
QK 216,44,156
VR 76,243,256
s_add_u32 s85, s80, 32768
QZ 220,48,144
buffer_load_dwordx4 a[188:191], v246, s[64:67], s86 offen offset:192
QK 220,52,148
VR 78,243,4352
VR 108,243,8448
QK 220,56,152
s_waitcnt vmcnt(8)
E 195
v_lshlrev_b32 v176, 16, v144
v_and_b32 v144, s83, v144
v_mul_f32 v176, s82, v176
QK 220,60,156
s_waitcnt vmcnt(4)
C 225,194
v_mul_f32 v144, s82, v144
v_cvt_pk_bf16_f32 v144, v176, v144
v_lshlrev_b32 v177, 16, v145
QZ 192,0,160
v_and_b32 v145, s83, v145
v_mul_f32 v177, s82, v177
v_mul_f32 v145, s82, v145
QK 192,4,164
E 196
E 198
v_cvt_pk_bf16_f32 v145, v177, v145
v_lshlrev_b32 v178, 16, v146
v_and_b32 v146, s83, v146
QK 192,8,168
buffer_load_dwordx4 v[160:163], v255, s[68:71], s85 offen offset:0
E 197
E 199
v_mul_f32 v178, s82, v178
v_mul_f32 v146, s82, v146
v_cvt_pk_bf16_f32 v146, v178, v146
QK 192,12,172
C 226,196
C 227,198
v_lshlrev_b32 v179, 16, v147
v_and_b32 v147, s83, v147
v_mul_f32 v179, s82, v179
v_mul_f32 v147, s82, v147
QZ 196,16,160
v_cvt_pk_bf16_f32 v147, v179, v147
v_lshlrev_b32 v180, 16, v148
v_and_b32 v148, s83, v148
QK 196,20,164
E 200
E 202
v_mul_f32 v180, s82, v180
v_mul_f32 v148, s82, v148
v_cvt_pk_bf16_f32 v148, v180, v148
QK 196,24,168
buffer_load_dwordx4 v[164:167], v255, s[68:71], s85 offen offset:1024
E 201
E 203
v_lshlrev_b32 v181, 16, v149
v_and_b32 v149, s83, v149
v_mul_f32 v181, s82, v181
QK 196,28,172
C 228,200
C 229,202
v_mul_f32 v149, s82, v149
v_cvt_pk_bf16_f32 v149, v181, v149
v_lshlrev_b32 v182, 16, v150
QZ 200,32,160
v_and_b32 v150, s83, v150
v_mul_f32 v182, s82, v182
v_mul_f32 v150, s82, v150
v_cvt_pk_bf16_f32 v150, v182, v150
QK 200,36,164
E 204
E 206
v_lshlrev_b32 v183, 16, v151
v_and_b32 v151, s83, v151
v_mul_f32 v183, s82, v183
QK 200,40,168
buffer_load_dwordx4 v[168:171], v255, s[68:71], s85 offen offset:2048
E 205
E 207
v_mul_f32 v151, s82, v151
v_cvt_pk_bf16_f32 v151, v183, v151
v_lshlrev_b32 v176, 16, v152
QK 200,44,172
C 230,204
C 231,206
v_and_b32 v152, s83, v152
v_mul_f32 v176, s82, v176
v_mul_f32 v152, s82, v152
QZ 204,48,160
v_cvt_pk_bf16_f32 v152, v176, v152
v_lshlrev_b32 v177, 16, v153
v_and_b32 v153, s83, v153
QK 204,52,164
v_mul_f32 v177, s82, v177
v_mul_f32 v153, s82, v153
v_cvt_pk_bf16_f32 v153, v177, v153
v_lshlrev_b32 v178, 16, v154
QK 204,56,168
buffer_load_dwordx4 v[172:175], v255, s[68:71], s85 offen offset:3072
v_and_b32 v154, s83, v154
v_mul_f32 v178, s82, v178
v_mul_f32 v154, s82, v154
v_lshlrev_b32 v179, 16, v155
QK 204,60,172
VR 110,243,12544
s_waitcnt lgkmcnt(1)
v_cvt_pk_bf16_f32 v154, v178, v154
v_and_b32 v155, s83, v155
v_mul_f32 v179, s82, v179
v_lshlrev_b32 v180, 16, v156
PZ 0,64,224
VR 80,242,512
VR 82,242,4608
v_mul_f32 v155, s82, v155
v_cvt_pk_bf16_f32 v155, v179, v155
v_and_b32 v156, s83, v156
v_mul_f32 v180, s82, v180
v_lshlrev_b32 v181, 16, v157
PV 0,96,228
s_add_u32 s84, s80, 16384
s_add_u32 m0, s80, 147456
VR 112,242,8704
VR 114,242,12800
v_mul_f32 v156, s82, v156
v_and_b32 v157, s83, v157
v_mul_f32 v181, s82, v181
v_lshlrev_b32 v182, 16, v158
PZ 4,68,224
buffer_load_dwordx4 v244, s[72:75], s84 offen offset:0  lds
VR 84,243,512
VR 86,243,4608
v_cvt_pk_bf16_f32 v156, v180, v156
v_mul_f32 v157, s82, v157
v_and_b32 v158, s83, v158
v_mul_f32 v182, s82, v182
v_lshlrev_b32 v183, 16, v159
PV 4,100,228
VR 116,243,8704
VR 118,243,12800
v_cvt_pk_bf16_f32 v157, v181, v157
v_mul_f32 v158, s82, v158
v_and_b32 v159, s83, v159
v_mul_f32 v183, s82, v183
PZ 8,72,224
VR 88,242,768
VR 90,242,4864
v_cvt_pk_bf16_f32 v158, v182, v158
v_mul_f32 v159, s82, v159
PV 8,104,228
VR 120,242,8960
VR 122,242,13056
s_waitcnt lgkmcnt(8)
v_cvt_pk_bf16_f32 v159, v183, v159
PZ 12,76,224
buffer_load_dwordx4 v245, s[72:75], s84 offen offset:1024  lds
VR 92,243,768
VR 94,243,4864
PV 12,108,228
VR 124,243,8960
VR 126,243,13056
PZ 16,80,224
ds_write_b128 v184, v[144:147] offset:16384
PV 16,112,228
s_waitcnt lgkmcnt(9)
ds_write_b128 v185, v[148:151] offset:16640
PZ 20,84,224
buffer_load_dwordx4 v244, s[72:75], s84 offen offset:2048  lds
ds_write_b128 v186, v[152:155] offset:16896
PV 20,116,228
s_waitcnt lgkmcnt(7)
ds_write_b128 v187, v[156:159] offset:17152
PZ 24,88,224
E 208
PV 24,120,228
s_waitcnt lgkmcnt(4)
PZ 28,92,224
buffer_load_dwordx4 v245, s[72:75], s84 offen offset:3072  lds
PV 28,124,228
E 210
SZ 224,224
E 209
E 211
SM 224,228
s_waitcnt vmcnt(8)
C 232,208
C 233,210
s_waitcnt lgkmcnt(0)
s_barrier
QZ 208,0,176
s_add_u32 s84, s80, 32768
s_add_u32 m0, s80, 65536
QK 208,4,180
buffer_load_dwordx4 v244, s[72:75], s84 offen offset:0  lds
E 212
E 214
QK 208,8,184
E 213
E 215
QK 208,12,188
C 234,212
C 235,214
QZ 212,16,176
buffer_load_dwordx4 v245, s[72:75], s84 offen offset:1024  lds
QK 212,20,180
E 216
E 218
QK 212,24,184
E 217
E 219
QK 212,28,188
C 236,216
C 237,218
QZ 216,32,176
buffer_load_dwordx4 v244, s[72:75], s84 offen offset:2048  lds
QK 216,36,180
E 220
E 222
QK 216,40,184
E 221
E 223
QK 216,44,188
C 238,220
C 239,222
QZ 220,48,176
buffer_load_dwordx4 v245, s[72:75], s84 offen offset:3072  lds
QK 220,52,180
KR 0,240,16384
s_mov_b32 s60, .LSTEP2-.LCODE0
QK 220,56,184
E 192
QK 220,60,188
KR 4,240,17408
PZ 32,64,232
E 194
PV 32,96,236
KR 8,240,18432
s_add_u32 s85, s80, 49152
PZ 36,68,232
E 193
PV 36,100,236
KR 12,240,19456
C 224,192
PZ 40,72,232
buffer_load_dwordx4 v[128:131], v255, s[68:71], s85 offen offset:0
PV 40,104,236
KR 16,240,20480
PZ 44,76,232
E 195
PV 44,108,236
KR 20,240,21504
C 225,194
PZ 48,80,232
E 196
PV 48,112,236
buffer_load_dwordx4 v[132:135], v255, s[68:71], s85 offen offset:1024
PZ 52,84,232
KR 24,240,22528
PV 52,116,236
E 198
PZ 56,88,232
KR 28,240,23552
PV 56,120,236
E 197
PZ 60,92,232
KR 32,240,24576
s_waitcnt lgkmcnt(5)
PV 60,124,236
buffer_load_dwordx4 v[136:139], v255, s[68:71], s85 offen offset:2048
SZ 228,232
KR 36,240,25600
C 226,196
SM 228,236
E 199
QZ 192,0,128
KR 40,240,26624
C 227,198
QK 192,4,132
E 200
QK 192,8,136
KR 44,240,27648
s_waitcnt lgkmcnt(4)
QK 192,12,140
buffer_load_dwordx4 v[140:143], v255, s[68:71], s85 offen offset:3072
QZ 196,16,128
KR 48,240,28672
s_add_u32 s85, s80, 65536
QK 196,20,132
E 202
KR 52,240,29696
QK 196,24,136
E 201
E 203
KR 56,240,30720
QK 196,28,140
C 228,200
C 229,202
KR 60,240,31744
s_waitcnt lgkmcnt(4)
s_waitcnt vmcnt(12)
v_lshlrev_b32 v176, 16, v160
QZ 200,32,128
v_and_b32 v160, s83, v160
s_add_u32 s84, s80, 49152
QK 200,36,132
E 204
E 206
v_mul_f32 v176, s82, v176
QK 200,40,136
s_add_u32 m0, s80, 81920
E 205
E 207
v_mul_f32 v160, s82, v160
QK 200,44,140
buffer_load_dwordx4 v244, s[72:75], s84 offen offset:0  lds
C 230,204
C 231,206
s_waitcnt lgkmcnt(0)
v_cvt_pk_bf16_f32 v160, v176, v160
QZ 204,48,128
v_lshlrev_b32 v177, 16, v161
v_and_b32 v161, s83, v161
QK 204,52,132
v_mul_f32 v177, s82, v177
v_mul_f32 v161, s82, v161
QK 204,56,136
v_cvt_pk_bf16_f32 v161, v177, v161
v_lshlrev_b32 v178, 16, v162
QK 204,60,140
v_and_b32 v162, s83, v162
v_mul_f32 v178, s82, v178
PZ 64,64,224
buffer_load_dwordx4 v245, s[72:75], s84 offen offset:1024  lds
PV 64,96,228
v_mul_f32 v162, s82, v162
v_cvt_pk_bf16_f32 v162, v178, v162
PZ 68,68,224
v_lshlrev_b32 v179, 16, v163
v_and_b32 v163, s83, v163
PV 68,100,228
E 208
PZ 72,72,224
E 210
PV 72,104,228
buffer_load_dwordx4 v244, s[72:75], s84 offen offset:2048  lds
PZ 76,76,224
E 209
PV 76,108,228
E 211
PZ 80,80,224
E 212
PV 80,112,228
v_mul_f32 v179, s82, v179
C 232,208
PZ 84,84,224
v_mul_f32 v163, s82, v163
s_waitcnt vmcnt(11)
PV 84,116,228
s_barrier
v_cvt_pk_bf16_f32 v163, v179, v163
PZ 88,88,224
v_lshlrev_b32 v180, 16, v164
C 233,210
PV 88,120,228
buffer_load_dwordx4 v245, s[72:75], s84 offen offset:3072  lds
v_and_b32 v164, s83, v164
PZ 92,92,224
v_mul_f32 v180, s82, v180
s_add_u32 s84, s80, 65536
PV 92,124,228
v_mul_f32 v164, s82, v164
s_add_u32 m0, s80, 98304
SZ 232,224
v_cvt_pk_bf16_f32 v164, v180, v164
v_lshlrev_b32 v181, 16, v165
SM 232,228
E 214
.LSTEP1:
QZ 208,0,144
v_and_b32 v165, s83, v165
v_mul_f32 v181, s82, v181
QK 208,4,148
buffer_load_dwordx4 v244, s[72:75], s84 offen offset:0  lds
QK 208,8,152
E 213
E 215
v_mul_f32 v165, s82, v165
QK 208,12,156
C 234,212
C 235,214
v_cvt_pk_bf16_f32 v165, v181, v165
QZ 212,16,144
v_lshlrev_b32 v182, 16, v166
v_and_b32 v166, s83, v166
QK 212,20,148
E 216
E 218
QK 212,24,152
E 217
E 219
QK 212,28,156
C 236,216
C 237,218
v_mul_f32 v182, s82, v182
QZ 216,32,144
v_mul_f32 v166, s82, v166
v_cvt_pk_bf16_f32 v166, v182, v166
QK 216,36,148
E 220
E 222
buffer_load_dwordx4 v245, s[72:75], s84 offen offset:1024  lds
QK 216,40,152
E 221
E 223
v_lshlrev_b32 v183, 16, v167
QK 216,44,156
C 238,220
C 239,222
v_and_b32 v167, s83, v167
QZ 220,48,144
v_mul_f32 v183, s82, v183
v_mul_f32 v167, s82, v167
QK 220,52,148
v_cvt_pk_bf16_f32 v167, v183, v167
v_lshlrev_b32 v176, 16, v168
QK 220,56,152
v_and_b32 v168, s83, v168
v_mul_f32 v176, s82, v176
QK 220,60,156
buffer_load_dwordx4 v244, s[72:75], s84 offen offset:2048  lds
PZ 96,64,232
v_mul_f32 v168, s82, v168
v_cvt_pk_bf16_f32 v168, v176, v168
PV 96,96,236
VR 64,242,16384
VR 66,242,20480
PZ 100,68,232
VR 96,242,24576
v_lshlrev_b32 v177, 16, v169
PV 100,100,236
VR 98,242,28672
v_and_b32 v169, s83, v169
PZ 104,72,232
VR 68,243,16384
v_mul_f32 v177, s82, v177
PV 104,104,236
buffer_load_dwordx4 v245, s[72:75], s84 offen offset:3072  lds
PZ 108,76,232
VR 70,243,20480
VR 100,243,24576
PV 108,108,236
VR 102,243,28672
v_mul_f32 v169, s82, v169
PZ 112,80,232
VR 72,242,16640
v_cvt_pk_bf16_f32 v169, v177, v169
PV 112,112,236
VR 74,242,20736
v_lshlrev_b32 v178, 16, v170
PZ 116,84,232
VR 104,242,24832
v_and_b32 v170, s83, v170
PV 116,116,236
VR 106,242,28928
v_mul_f32 v178, s82, v178
PZ 120,88,232
buffer_load_dwordx4 v[144:147], v255, s[68:71], s85 offen offset:0
PV 120,120,236
v_mul_f32 v170, s82, v170
VR 76,243,16640
PZ 124,92,232
v_cvt_pk_bf16_f32 v170, v178, v170
VR 78,243,20736
PV 124,124,236
E 192
E 194
v_lshlrev_b32 v179, 16, v171
SZ 236,232
E 193
E 195
v_and_b32 v171, s83, v171
SM 236,236
C 224,192
C 225,194
v_mul_f32 v179, s82, v179
QZ 192,0,160
v_mul_f32 v171, s82, v171
VR 108,243,24832
QK 192,4,164
E 196
E 198
v_cvt_pk_bf16_f32 v171, v179, v171
QK 192,8,168
E 197
E 199
v_lshlrev_b32 v180, 16, v172
QK 192,12,172
C 226,196
C 227,198
buffer_load_dwordx4 v[148:151], v255, s[68:71], s85 offen offset:1024
v_and_b32 v172, s83, v172
QZ 196,16,160
v_mul_f32 v180, s82, v180
VR 110,243,28928
QK 196,20,164
E 200
E 202
QK 196,24,168
E 201
E 203
v_mul_f32 v172, s82, v172
QK 196,28,172
C 228,200
C 229,202
v_cvt_pk_bf16_f32 v172, v180, v172
QZ 200,32,160
v_lshlrev_b32 v181, 16, v173
VR 80,242,16896
QK 200,36,164
E 204
E 206
v_and_b32 v173, s83, v173
QK 200,40,168
E 205
E 207
buffer_load_dwordx4 v[152:155], v255, s[68:71], s85 offen offset:2048
v_mul_f32 v181, s82, v181
QK 200,44,172
C 230,204
C 231,206
v_mul_f32 v173, s82, v173
QZ 204,48,160
v_cvt_pk_bf16_f32 v173, v181, v173
s_waitcnt lgkmcnt(1)
QK 204,52,164
v_lshlrev_b32 v182, 16, v174
VR 82,242,20992
QK 204,56,168
v_and_b32 v174, s83, v174
VR 112,242,25088
QK 204,60,172
v_mul_f32 v182, s82, v182
VR 114,242,29184
PV 0,64,224
v_mul_f32 v174, s82, v174
VR 84,243,16896
PV 0,96,228
buffer_load_dwordx4 v[156:159], v255, s[68:71], s85 offen offset:3072
v_cvt_pk_bf16_f32 v174, v182, v174
PV 4,68,224
VR 86,243,20992
v_lshlrev_b32 v183, 16, v175
PV 4,100,228
VR 116,243,25088
VR 118,243,29184
v_and_b32 v175, s83, v175
v_mul_f32 v183, s82, v183
PV 8,72,224
VR 88,242,17152
VR 90,242,21248
v_mul_f32 v175, s82, v175
PV 8,104,228
VR 120,242,25344
VR 122,242,29440
v_cvt_pk_bf16_f32 v175, v183, v175
PV 12,76,224
VR 92,243,17152
VR 94,243,21248
PV 12,108,228
VR 124,243,25344
VR 126,243,29440
s_waitcnt lgkmcnt(11)
PV 16,80,224
buffer_load_dword a244, v252, s[56:59], s60 offen
ds_write_b128 v184, v[160:163] offset:32768
PV 16,112,228
s_waitcnt lgkmcnt(9)
ds_write_b128 v185, v[164:167] offset:33024
PV 20,84,224
ds_write_b128 v186, v[168:171] offset:33280
PV 20,116,228
s_waitcnt lgkmcnt(7)
ds_write_b128 v187, v[172:175] offset:33536
PV 24,88,224
E 208
PV 24,120,228
s_waitcnt lgkmcnt(4)
s_add_u32 s84, s80, 81920
PV 28,92,224
E 210
PV 28,124,228
E 209
SM 224,224
E 211
SM 224,228
C 232,208
C 233,210
s_waitcnt vmcnt(17)
s_waitcnt lgkmcnt(0)
s_barrier
s_add_u32 m0, s80, 114688
QZ 208,0,176
buffer_load_dwordx4 v244, s[72:75], s84 offen offset:0  lds
QK 208,4,180
E 212
E 214
QK 208,8,184
E 213
E 215
QK 208,12,188
C 234,212
C 235,214
s_waitcnt vmcnt(14)
v_lshlrev_b32 v176, 16, v128
QZ 212,16,176
E 216
QK 212,20,180
E 218
v_and_b32 v128, s83, v128
QK 212,24,184
E 217
E 219
v_mul_f32 v176, s82, v176
QK 212,28,188
C 236,216
C 237,218
buffer_load_dwordx4 v245, s[72:75], s84 offen offset:1024  lds
QZ 216,32,176
v_mul_f32 v128, s82, v128
s_add_u32 s85, s80, 81920
QK 216,36,180
E 220
E 222
v_cvt_pk_bf16_f32 v128, v176, v128
QK 216,40,184
E 221
E 223
QK 216,44,188
C 238,220
C 239,222
v_lshlrev_b32 v177, 16, v129
QZ 220,48,176
v_and_b32 v129, s83, v129
KR 0,240,32768
QK 220,52,180
buffer_load_dwordx4 v244, s[72:75], s84 offen offset:2048  lds
QK 220,56,184
v_mul_f32 v177, s82, v177
KR 4,240,33792
QK 220,60,188
v_mul_f32 v129, s82, v129
s_mov_b32 s60, .LSTEP3-.LCODE0
PV 32,64,232
KR 8,240,34816
v_cvt_pk_bf16_f32 v129, v177, v129
PV 32,96,236
v_lshlrev_b32 v178, 16, v130
v_and_b32 v130, s83, v130
PV 36,68,232
KR 12,240,35840
v_mul_f32 v178, s82, v178
PV 36,100,236
buffer_load_dwordx4 v245, s[72:75], s84 offen offset:3072  lds
PV 40,72,232
KR 16,240,36864
v_mul_f32 v130, s82, v130
PV 40,104,236
E 192
PV 44,76,232
KR 20,240,37888
v_cvt_pk_bf16_f32 v130, v178, v130
PV 44,108,236
E 194
PV 48,80,232
KR 24,240,38912
v_lshlrev_b32 v179, 16, v131
PV 48,112,236
E 193
PV 52,84,232
buffer_load_dwordx4 v[160:163], v255, s[68:71], s85 offen offset:0
PV 52,116,236
KR 28,240,39936
C 224,192
PV 56,88,232
v_and_b32 v131, s83, v131
v_mul_f32 v179, s82, v179
PV 56,120,236
KR 32,240,40960
v_mul_f32 v131, s82, v131
PV 60,92,232
E 195
PV 60,124,236
KR 36,240,41984
C 225,194
SM 228,232
v_cvt_pk_bf16_f32 v131, v179, v131
s_waitcnt lgkmcnt(4)
SM 228,236
buffer_load_dwordx4 v[164:167], v255, s[68:71], s85 offen offset:1024
QZ 192,0,128
v_lshlrev_b32 v180, 16, v132
KR 40,240,43008
QK 192,4,132
E 196
E 198
v_and_b32 v132, s83, v132
QK 192,8,136
E 197
E 199
QK 192,12,140
C 226,196
C 227,198
KR 44,240,44032
s_waitcnt lgkmcnt(3)
v_mul_f32 v180, s82, v180
QZ 196,16,128
KR 48,240,45056
v_mul_f32 v132, s82, v132
QK 196,20,132
E 200
E 202
KR 52,240,46080
QK 196,24,136
E 201
E 203
KR 56,240,47104
v_cvt_pk_bf16_f32 v132, v180, v132
QK 196,28,140
C 228,200
C 229,202
KR 60,240,48128
s_waitcnt lgkmcnt(4)
v_lshlrev_b32 v181, 16, v133
QZ 200,32,128
buffer_load_dwordx4 v[168:171], v255, s[68:71], s85 offen offset:2048
QK 200,36,132
E 204
E 206
v_and_b32 v133, s83, v133
QK 200,40,136
E 205
E 207
QK 200,44,140
C 230,204
C 231,206
s_waitcnt lgkmcnt(0)
v_mul_f32 v181, s82, v181
QZ 204,48,128
v_mul_f32 v133, s82, v133
v_cvt_pk_bf16_f32 v133, v181, v133
QK 204,52,132
v_lshlrev_b32 v182, 16, v134
v_and_b32 v134, s83, v134
QK 204,56,136
buffer_load_dwordx4 v[172:175], v255, s[68:71], s85 offen offset:3072
QK 204,60,140
v_mul_f32 v182, s82, v182
v_mul_f32 v134, s82, v134
PV 64,64,224
v_cvt_pk_bf16_f32 v134, v182, v134
v_lshlrev_b32 v183, 16, v135
PV 64,96,228
v_and_b32 v135, s83, v135
v_mul_f32 v183, s82, v183
PV 68,68,224
v_mul_f32 v135, s82, v135
v_cvt_pk_bf16_f32 v135, v183, v135
PV 68,100,228
E 208
PV 72,72,224
buffer_load_dword a244, v252, s[56:59], s60 offen
PV 72,104,228
E 210
PV 76,76,224
E 209
PV 76,108,228
E 211
PV 80,80,224
E 212
PV 80,112,228
E 214
PV 84,84,224
E 213
PV 84,116,228
E 215
PV 88,88,224
E 216
PV 88,120,228
E 218
PV 92,92,224
E 217
PV 92,124,228
v_lshlrev_b32 v176, 16, v136
C 232,208
SM 232,224
v_and_b32 v136, s83, v136
C 233,210
SM 232,228
E 219
.LSTEP2:
QZ 208,0,144
v_mul_f32 v176, s82, v176
C 234,212
QK 208,4,148
v_mul_f32 v136, s82, v136
C 235,214
QK 208,8,152
E 220
QK 208,12,156
v_cvt_pk_bf16_f32 v136, v176, v136
C 236,216
QZ 212,16,144
v_lshlrev_b32 v177, 16, v137
C 237,218
QK 212,20,148
E 222
QK 212,24,152
v_and_b32 v137, s83, v137
v_mul_f32 v177, s82, v177
QK 212,28,156
E 221
QZ 216,32,144
E 223
QK 216,36,148
v_mul_f32 v137, s82, v137
C 238,220
QK 216,40,152
v_cvt_pk_bf16_f32 v137, v177, v137
C 239,222
QK 216,44,156
v_lshlrev_b32 v178, 16, v138
v_and_b32 v138, s83, v138
QZ 220,48,144
v_mul_f32 v178, s82, v178
v_mul_f32 v138, s82, v138
QK 220,52,148
v_cvt_pk_bf16_f32 v138, v178, v138
v_lshlrev_b32 v179, 16, v139
QK 220,56,152
v_and_b32 v139, s83, v139
v_mul_f32 v179, s82, v179
QK 220,60,156
v_mul_f32 v139, s82, v139
v_cvt_pk_bf16_f32 v139, v179, v139
PV 96,64,232
v_lshlrev_b32 v180, 16, v140
v_and_b32 v140, s83, v140
PV 96,96,236
VR 64,188,0
VR 66,188,4096
PV 100,68,232
VR 96,188,8192
v_mul_f32 v180, s82, v180
PV 100,100,236
VR 98,188,12288
v_mul_f32 v140, s82, v140
PV 104,72,232
VR 68,189,0
v_cvt_pk_bf16_f32 v140, v180, v140
PV 104,104,236
VR 70,189,4096
v_lshlrev_b32 v181, 16, v141
PV 108,76,232
VR 100,189,8192
v_and_b32 v141, s83, v141
PV 108,108,236
VR 102,189,12288
v_mul_f32 v181, s82, v181
PV 112,80,232
VR 72,188,256
v_mul_f32 v141, s82, v141
PV 112,112,236
VR 74,188,4352
v_cvt_pk_bf16_f32 v141, v181, v141
PV 116,84,232
VR 104,188,8448
v_lshlrev_b32 v182, 16, v142
PV 116,116,236
VR 106,188,12544
VR 76,189,256
PV 120,88,232
VR 78,189,4352
v_and_b32 v142, s83, v142
PV 120,120,236
VR 108,189,8448
v_mul_f32 v182, s82, v182
PV 124,92,232
VR 110,189,12544
v_mul_f32 v142, s82, v142
PV 124,124,236
E 192
E 194
SM 236,232
E 193
E 195
SM 236,236
C 224,192
C 225,194
QZ 192,0,160
VR 80,188,512
VR 82,188,4608
QK 192,4,164
E 196
E 198
QK 192,8,168
E 197
E 199
QK 192,12,172
C 226,196
C 227,198
QZ 196,16,160
VR 112,188,8704
VR 114,188,12800
QK 196,20,164
E 200
E 202
QK 196,24,168
E 201
E 203
QK 196,28,172
C 228,200
C 229,202
QZ 200,32,160
VR 84,189,512
VR 86,189,4608
QK 200,36,164
E 204
E 206
QK 200,40,168
E 205
E 207
QK 200,44,172
C 230,204
C 231,206
QZ 204,48,160
VR 116,189,8704
s_waitcnt lgkmcnt(3)
QK 204,52,164
VR 118,189,12800
VR 88,188,768
QK 204,56,168
VR 90,188,4864
v_cvt_pk_bf16_f32 v142, v182, v142
QK 204,60,172
VR 120,188,8960
v_lshlrev_b32 v183, 16, v143
PV 0,64,224
VR 122,188,13056
v_and_b32 v143, s83, v143
PV 0,96,228
VR 92,189,768
v_mul_f32 v183, s82, v183
PV 4,68,224
VR 94,189,4864
v_mul_f32 v143, s82, v143
PV 4,100,228
VR 124,189,8960
v_cvt_pk_bf16_f32 v143, v183, v143
PV 8,72,224
VR 126,189,13056
PV 8,104,228
E 208
PV 12,76,224
E 210
PV 12,108,228
E 209
PV 16,80,224
ds_write_b128 v184, v[128:131] offset:49152
PV 16,112,228
s_waitcnt lgkmcnt(6)
ds_write_b128 v185, v[132:135] offset:49408
PV 20,84,224
ds_write_b128 v186, v[136:139] offset:49664
PV 20,116,228
s_waitcnt lgkmcnt(6)
ds_write_b128 v187, v[140:143] offset:49920
PV 24,88,224
E 211
PV 24,120,228
s_waitcnt lgkmcnt(4)
C 232,208
PV 28,92,224
E 212
PV 28,124,228
E 214
SM 224,224
E 213
SM 224,228
C 233,210
s_waitcnt vmcnt(18)
s_waitcnt lgkmcnt(0)
s_barrier
s_add_u32 s84, s80, 98304
s_add_u32 m0, s80, 131072
QZ 208,0,176
buffer_load_dwordx4 v244, s[72:75], s84 offen offset:0  lds
QK 208,4,180
E 215
QK 208,8,184
E 216
QK 208,12,188
C 234,212
C 235,214
s_waitcnt vmcnt(11)
v_lshlrev_b32 v176, 16, v144
QZ 212,16,176
E 218
QK 212,20,180
v_and_b32 v144, s83, v144
KR 0,240,49152
QK 212,24,184
E 217
E 219
v_mul_f32 v176, s82, v176
QK 212,28,188
C 236,216
C 237,218
buffer_load_dwordx4 v245, s[72:75], s84 offen offset:1024  lds
QZ 216,32,176
v_mul_f32 v144, s82, v144
s_add_u32 s85, s80, 98304
QK 216,36,180
E 220
E 222
v_cvt_pk_bf16_f32 v144, v176, v144
QK 216,40,184
E 221
E 223
QK 216,44,188
C 238,220
C 239,222
v_lshlrev_b32 v177, 16, v145
QZ 220,48,176
v_and_b32 v145, s83, v145
KR 4,240,50176
QK 220,52,180
buffer_load_dwordx4 v244, s[72:75], s84 offen offset:2048  lds
QK 220,56,184
v_mul_f32 v177, s82, v177
KR 8,240,51200
QK 220,60,188
v_mul_f32 v145, s82, v145
s_mov_b32 s60, .LSTEP4-.LCODE0
PV 32,64,232
KR 12,240,52224
v_add_u32 v184, 0x10000, v184
PV 32,96,236
v_cvt_pk_bf16_f32 v145, v177, v145
v_add_u32 v185, 0x10000, v185
PV 36,68,232
v_lshlrev_b32 v178, 16, v146
KR 16,240,53248
PV 36,100,236
buffer_load_dwordx4 v245, s[72:75], s84 offen offset:3072  lds
PV 40,72,232
v_and_b32 v146, s83, v146
KR 20,240,54272
PV 40,104,236
v_mul_f32 v178, s82, v178
v_add_u32 v186, 0x10000, v186
PV 44,76,232
KR 24,240,55296
v_add_u32 v187, 0x10000, v187
PV 44,108,236
v_mul_f32 v146, s82, v146
v_cvt_pk_bf16_f32 v146, v178, v146
PV 48,80,232
KR 28,240,56320
v_lshlrev_b32 v179, 16, v147
PV 48,112,236
E 192
PV 52,84,232
buffer_load_dwordx4 v[128:131], v255, s[68:71], s85 offen offset:0
PV 52,116,236
KR 32,240,57344
v_and_b32 v147, s83, v147
PV 56,88,232
E 194
PV 56,120,236
v_mul_f32 v179, s82, v179
KR 36,240,58368
PV 60,92,232
E 193
PV 60,124,236
v_mul_f32 v147, s82, v147
C 224,192
SM 228,232
E 195
v_cvt_pk_bf16_f32 v147, v179, v147
SM 228,236
C 225,194
s_waitcnt lgkmcnt(2)
QZ 192,0,128
v_lshlrev_b32 v180, 16, v148
KR 40,240,59392
QK 192,4,132
E 196
E 198
buffer_load_dwordx4 v[132:135], v255, s[68:71], s85 offen offset:1024
v_and_b32 v148, s83, v148
QK 192,8,136
E 197
E 199
QK 192,12,140
C 226,196
C 227,198
KR 44,240,60416
v_mul_f32 v180, s82, v180
QZ 196,16,128
KR 48,240,61440
v_mul_f32 v148, s82, v148
QK 196,20,132
E 200
E 202
KR 52,240,62464
QK 196,24,136
E 201
E 203
KR 56,240,63488
v_cvt_pk_bf16_f32 v148, v180, v148
QK 196,28,140
C 228,200
C 229,202
KR 60,240,64512
s_waitcnt lgkmcnt(4)
v_lshlrev_b32 v181, 16, v149
QZ 200,32,128
buffer_load_dwordx4 v[136:139], v255, s[68:71], s85 offen offset:2048
QK 200,36,132
E 204
E 206
v_and_b32 v149, s83, v149
QK 200,40,136
E 205
E 207
QK 200,44,140
C 230,204
C 231,206
s_waitcnt lgkmcnt(0)
v_mul_f32 v181, s82, v181
QZ 204,48,128
v_mul_f32 v149, s82, v149
v_cvt_pk_bf16_f32 v149, v181, v149
QK 204,52,132
v_lshlrev_b32 v182, 16, v150
v_and_b32 v150, s83, v150
QK 204,56,136
buffer_load_dwordx4 v[140:143], v255, s[68:71], s85 offen offset:3072
QK 204,60,140
v_mul_f32 v182, s82, v182
v_mul_f32 v150, s82, v150
PV 64,64,224
v_cvt_pk_bf16_f32 v150, v182, v150
v_lshlrev_b32 v183, 16, v151
PV 64,96,228
v_and_b32 v151, s83, v151
v_mul_f32 v183, s82, v183
PV 68,68,224
v_mul_f32 v151, s82, v151
v_cvt_pk_bf16_f32 v151, v183, v151
PV 68,100,228
E 208
PV 72,72,224
buffer_load_dword a244, v252, s[56:59], s60 offen
PV 72,104,228
E 210
PV 76,76,224
E 209
PV 76,108,228
E 211
PV 80,80,224
E 212
PV 80,112,228
E 214
PV 84,84,224
E 213
PV 84,116,228
E 215
PV 88,88,224
E 216
PV 88,120,228
E 218
PV 92,92,224
E 217
PV 92,124,228
v_lshlrev_b32 v176, 16, v152
C 232,208
SM 232,224
v_and_b32 v152, s83, v152
C 233,210
SM 232,228
E 219
.LSTEP3:
QZ 208,0,144
v_mul_f32 v176, s82, v176
C 234,212
QK 208,4,148
v_mul_f32 v152, s82, v152
C 235,214
QK 208,8,152
E 220
QK 208,12,156
v_cvt_pk_bf16_f32 v152, v176, v152
C 236,216
QZ 212,16,144
v_lshlrev_b32 v177, 16, v153
C 237,218
QK 212,20,148
E 222
QK 212,24,152
v_and_b32 v153, s83, v153
v_mul_f32 v177, s82, v177
QK 212,28,156
E 221
QZ 216,32,144
E 223
QK 216,36,148
v_mul_f32 v153, s82, v153
C 238,220
QK 216,40,152
v_cvt_pk_bf16_f32 v153, v177, v153
C 239,222
QK 216,44,156
v_lshlrev_b32 v178, 16, v154
v_and_b32 v154, s83, v154
QZ 220,48,144
v_mul_f32 v178, s82, v178
v_mul_f32 v154, s82, v154
QK 220,52,148
v_cvt_pk_bf16_f32 v154, v178, v154
v_lshlrev_b32 v179, 16, v155
QK 220,56,152
v_and_b32 v155, s83, v155
v_mul_f32 v179, s82, v179
QK 220,60,156
v_mul_f32 v155, s82, v155
v_cvt_pk_bf16_f32 v155, v179, v155
PV 96,64,232
v_lshlrev_b32 v180, 16, v156
v_and_b32 v156, s83, v156
PV 96,96,236
VR 64,188,16384
VR 66,188,20480
PV 100,68,232
VR 96,188,24576
v_mul_f32 v180, s82, v180
PV 100,100,236
VR 98,188,28672
v_mul_f32 v156, s82, v156
PV 104,72,232
VR 68,189,16384
v_cvt_pk_bf16_f32 v156, v180, v156
PV 104,104,236
VR 70,189,20480
v_lshlrev_b32 v181, 16, v157
PV 108,76,232
VR 100,189,24576
v_and_b32 v157, s83, v157
PV 108,108,236
VR 102,189,28672
v_mul_f32 v181, s82, v181
PV 112,80,232
VR 72,188,16640
v_mul_f32 v157, s82, v157
PV 112,112,236
VR 74,188,20736
v_cvt_pk_bf16_f32 v157, v181, v157
PV 116,84,232
VR 104,188,24832
v_lshlrev_b32 v182, 16, v158
PV 116,116,236
VR 106,188,28928
VR 76,189,16640
PV 120,88,232
VR 78,189,20736
v_and_b32 v158, s83, v158
PV 120,120,236
VR 108,189,24832
v_mul_f32 v182, s82, v182
PV 124,92,232
VR 110,189,28928
v_mul_f32 v158, s82, v158
PV 124,124,236
E 192
E 194
SM 236,232
E 193
E 195
SM 236,236
C 224,192
C 225,194
QZ 192,0,160
VR 80,188,16896
VR 82,188,20992
QK 192,4,164
E 196
E 198
QK 192,8,168
E 197
E 199
QK 192,12,172
C 226,196
C 227,198
QZ 196,16,160
VR 112,188,25088
VR 114,188,29184
QK 196,20,164
E 200
E 202
QK 196,24,168
E 201
E 203
QK 196,28,172
C 228,200
C 229,202
QZ 200,32,160
VR 84,189,16896
VR 86,189,20992
QK 200,36,164
E 204
E 206
QK 200,40,168
E 205
E 207
QK 200,44,172
C 230,204
C 231,206
QZ 204,48,160
VR 116,189,25088
s_waitcnt lgkmcnt(3)
QK 204,52,164
VR 118,189,29184
VR 88,188,17152
QK 204,56,168
VR 90,188,21248
v_cvt_pk_bf16_f32 v158, v182, v158
QK 204,60,172
VR 120,188,25344
v_lshlrev_b32 v183, 16, v159
PV 0,64,224
VR 122,188,29440
v_and_b32 v159, s83, v159
PV 0,96,228
VR 92,189,17152
v_mul_f32 v183, s82, v183
PV 4,68,224
VR 94,189,21248
v_mul_f32 v159, s82, v159
PV 4,100,228
VR 124,189,25344
v_cvt_pk_bf16_f32 v159, v183, v159
PV 8,72,224
VR 126,189,29440
PV 8,104,228
E 208
PV 12,76,224
E 210
PV 12,108,228
E 209
PV 16,80,224
ds_write_b128 v184, v[144:147] offset:0
PV 16,112,228
s_waitcnt lgkmcnt(6)
ds_write_b128 v185, v[148:151] offset:256
PV 20,84,224
ds_write_b128 v186, v[152:155] offset:512
PV 20,116,228
s_waitcnt lgkmcnt(6)
ds_write_b128 v187, v[156:159] offset:768
PV 24,88,224
E 211
PV 24,120,228
s_waitcnt lgkmcnt(4)
C 232,208
PV 28,92,224
E 212
PV 28,124,228
E 214
SM 224,224
E 213
SM 224,228
C 233,210
s_waitcnt lgkmcnt(0)
s_barrier
s_add_u32 s84, s80, 114688
s_add_u32 m0, s80, 147456
QZ 208,0,176
buffer_load_dwordx4 v244, s[72:75], s84 offen offset:0  lds
QK 208,4,180
E 215
QK 208,8,184
E 216
QK 208,12,188
C 234,212
C 235,214
s_waitcnt vmcnt(11)
v_lshlrev_b32 v176, 16, v160
QZ 212,16,176
E 218
QK 212,20,180
v_and_b32 v160, s83, v160
KR 0,241,0
QK 212,24,184
E 217
E 219
v_mul_f32 v176, s82, v176
QK 212,28,188
C 236,216
C 237,218
buffer_load_dwordx4 v245, s[72:75], s84 offen offset:1024  lds
QZ 216,32,176
v_mul_f32 v160, s82, v160
s_add_u32 s85, s80, 114688
QK 216,36,180
E 220
E 222
v_cvt_pk_bf16_f32 v160, v176, v160
QK 216,40,184
E 221
E 223
QK 216,44,188
C 238,220
C 239,222
v_lshlrev_b32 v177, 16, v161
QZ 220,48,176
v_and_b32 v161, s83, v161
KR 4,241,1024
QK 220,52,180
buffer_load_dwordx4 v244, s[72:75], s84 offen offset:2048  lds
QK 220,56,184
v_mul_f32 v177, s82, v177
KR 8,241,2048
QK 220,60,188
v_mul_f32 v161, s82, v161
s_mov_b32 s60, .LSTEP5-.LCODE0
PV 32,64,232
KR 12,241,3072
v_cvt_pk_bf16_f32 v161, v177, v161
PV 32,96,236
v_lshlrev_b32 v178, 16, v162
v_and_b32 v162, s83, v162
PV 36,68,232
KR 16,241,4096
v_mul_f32 v178, s82, v178
PV 36,100,236
buffer_load_dwordx4 v245, s[72:75], s84 offen offset:3072  lds
PV 40,72,232
KR 20,241,5120
v_mul_f32 v162, s82, v162
PV 40,104,236
E 192
PV 44,76,232
KR 24,241,6144
v_cvt_pk_bf16_f32 v162, v178, v162
PV 44,108,236
E 194
PV 48,80,232
KR 28,241,7168
v_lshlrev_b32 v179, 16, v163
PV 48,112,236
E 193
PV 52,84,232
buffer_load_dwordx4 v[144:147], v255, s[68:71], s85 offen offset:0
PV 52,116,236
KR 32,241,8192
C 224,192
PV 56,88,232
v_and_b32 v163, s83, v163
v_mul_f32 v179, s82, v179
PV 56,120,236
KR 36,241,9216
v_mul_f32 v163, s82, v163
PV 60,92,232
E 195
PV 60,124,236
KR 40,241,10240
C 225,194
SM 228,232
v_cvt_pk_bf16_f32 v163, v179, v163
s_waitcnt lgkmcnt(4)
SM 228,236
buffer_load_dwordx4 v[148:151], v255, s[68:71], s85 offen offset:1024
QZ 192,0,128
v_lshlrev_b32 v180, 16, v164
s_waitcnt lgkmcnt(3)
QK 192,4,132
E 196
E 198
v_and_b32 v164, s83, v164
QK 192,8,136
E 197
E 199
QK 192,12,140
C 226,196
C 227,198
KR 44,241,11264
v_mul_f32 v180, s82, v180
QZ 196,16,128
KR 48,241,12288
v_mul_f32 v164, s82, v164
QK 196,20,132
E 200
E 202
KR 52,241,13312
QK 196,24,136
E 201
E 203
KR 56,241,14336
v_cvt_pk_bf16_f32 v164, v180, v164
QK 196,28,140
C 228,200
C 229,202
KR 60,241,15360
s_waitcnt lgkmcnt(4)
v_lshlrev_b32 v181, 16, v165
QZ 200,32,128
buffer_load_dwordx4 v[152:155], v255, s[68:71], s85 offen offset:2048
QK 200,36,132
E 204
E 206
v_and_b32 v165, s83, v165
QK 200,40,136
E 205
E 207
QK 200,44,140
C 230,204
C 231,206
s_waitcnt lgkmcnt(0)
v_mul_f32 v181, s82, v181
QZ 204,48,128
v_mul_f32 v165, s82, v165
v_cvt_pk_bf16_f32 v165, v181, v165
QK 204,52,132
v_lshlrev_b32 v182, 16, v166
v_and_b32 v166, s83, v166
QK 204,56,136
buffer_load_dwordx4 v[156:159], v255, s[68:71], s85 offen offset:3072
QK 204,60,140
v_mul_f32 v182, s82, v182
v_mul_f32 v166, s82, v166
PV 64,64,224
v_cvt_pk_bf16_f32 v166, v182, v166
v_lshlrev_b32 v183, 16, v167
PV 64,96,228
s_add_u32 s86, s81, 65536
v_and_b32 v167, s83, v167
PV 68,68,224
v_mul_f32 v183, s82, v183
v_mul_f32 v167, s82, v167
PV 68,100,228
E 208
PV 72,72,224
buffer_load_dword a244, v252, s[56:59], s60 offen
PV 72,104,228
E 210
PV 76,76,224
E 209
PV 76,108,228
E 211
PV 80,80,224
E 212
PV 80,112,228
E 214
PV 84,84,224
E 213
PV 84,116,228
buffer_load_dwordx4 a[192:195], v246, s[64:67], s86 offen offset:0
PV 88,88,224
E 215
PV 88,120,228
v_cvt_pk_bf16_f32 v167, v183, v167
C 232,208
PV 92,92,224
E 216
PV 92,124,228
v_lshlrev_b32 v176, 16, v168
C 233,210
SM 232,224
v_and_b32 v168, s83, v168
C 234,212
SM 232,228
E 218
.LSTEP4:
QZ 208,0,144
v_mul_f32 v176, s82, v176
C 235,214
QK 208,4,148
v_mul_f32 v168, s82, v168
v_cvt_pk_bf16_f32 v168, v176, v168
QK 208,8,152
buffer_load_dwordx4 a[196:199], v246, s[64:67], s86 offen offset:64
QK 208,12,156
E 217
QZ 212,16,144
v_lshlrev_b32 v177, 16, v169
C 236,216
QK 212,20,148
E 219
QK 212,24,152
v_and_b32 v169, s83, v169
C 237,218
QK 212,28,156
v_mul_f32 v177, s82, v177
v_mul_f32 v169, s82, v169
QZ 216,32,144
buffer_load_dwordx4 a[200:203], v246, s[64:67], s86 offen offset:128
QK 216,36,148
E 220
E 222
QK 216,40,152
E 221
E 223
v_cvt_pk_bf16_f32 v169, v177, v169
QK 216,44,156
C 238,220
C 239,222
QZ 220,48,144
v_lshlrev_b32 v178, 16, v170
v_and_b32 v170, s83, v170
QK 220,52,148
v_mul_f32 v178, s82, v178
v_mul_f32 v170, s82, v170
QK 220,56,152
buffer_load_dwordx4 a[204:207], v246, s[64:67], s86 offen offset:192
QK 220,60,156
v_cvt_pk_bf16_f32 v170, v178, v170
v_lshlrev_b32 v179, 16, v171
PV 96,64,232
v_and_b32 v171, s83, v171
v_mul_f32 v179, s82, v179
PV 96,96,236
VR 64,188,32768
VR 66,188,36864
PV 100,68,232
VR 96,188,40960
v_mul_f32 v171, s82, v171
PV 100,100,236
VR 98,188,45056
v_cvt_pk_bf16_f32 v171, v179, v171
PV 104,72,232
VR 68,189,32768
v_lshlrev_b32 v180, 16, v172
PV 104,104,236
VR 70,189,36864
v_and_b32 v172, s83, v172
PV 108,76,232
VR 100,189,40960
v_mul_f32 v180, s82, v180
PV 108,108,236
VR 102,189,45056
v_mul_f32 v172, s82, v172
PV 112,80,232
VR 72,188,33024
v_cvt_pk_bf16_f32 v172, v180, v172
PV 112,112,236
VR 74,188,37120
v_lshlrev_b32 v181, 16, v173
PV 116,84,232
VR 104,188,41216
v_and_b32 v173, s83, v173
PV 116,116,236
VR 106,188,45312
VR 76,189,33024
PV 120,88,232
VR 78,189,37120
v_mul_f32 v181, s82, v181
PV 120,120,236
VR 108,189,41216
v_mul_f32 v173, s82, v173
PV 124,92,232
VR 110,189,45312
v_cvt_pk_bf16_f32 v173, v181, v173
PV 124,124,236
E 192
E 194
SM 236,232
E 193
E 195
SM 236,236
C 224,192
C 225,194
QZ 192,0,160
VR 80,188,33280
VR 82,188,37376
QK 192,4,164
E 196
E 198
QK 192,8,168
E 197
E 199
QK 192,12,172
C 226,196
C 227,198
QZ 196,16,160
VR 112,188,41472
VR 114,188,45568
QK 196,20,164
E 200
E 202
QK 196,24,168
E 201
E 203
QK 196,28,172
C 228,200
C 229,202
QZ 200,32,160
VR 84,189,33280
VR 86,189,37376
QK 200,36,164
E 204
E 206
v_lshlrev_b32 v182, 16, v174
QK 200,40,168
E 205
E 207
v_and_b32 v174, s83, v174
QK 200,44,172
C 230,204
C 231,206
QZ 204,48,160
v_mul_f32 v182, s82, v182
s_waitcnt lgkmcnt(2)
QK 204,52,164
v_mul_f32 v174, s82, v174
VR 116,189,41472
QK 204,56,168
VR 118,189,45568
VR 88,188,33536
QK 204,60,172
v_cvt_pk_bf16_f32 v174, v182, v174
VR 90,188,37632
PV 0,64,224
v_lshlrev_b32 v183, 16, v175
VR 120,188,41728
PV 0,96,228
VR 122,188,45824
v_and_b32 v175, s83, v175
PV 4,68,224
VR 92,189,33536
v_mul_f32 v183, s82, v183
PV 4,100,228
VR 94,189,37632
v_mul_f32 v175, s82, v175
PV 8,72,224
VR 124,189,41728
v_cvt_pk_bf16_f32 v175, v183, v175
PV 8,104,228
VR 126,189,45824
PV 12,76,224
E 208
PV 12,108,228
E 210
PV 16,80,224
ds_write_b128 v184, v[160:163] offset:16384
PV 16,112,228
s_waitcnt lgkmcnt(7)
ds_write_b128 v185, v[164:167] offset:16640
PV 20,84,224
ds_write_b128 v186, v[168:171] offset:16896
PV 20,116,228
s_waitcnt lgkmcnt(7)
ds_write_b128 v187, v[172:175] offset:17152
PV 24,88,224
E 209
PV 24,120,228
s_waitcnt lgkmcnt(4)
C 232,208
PV 28,92,224
E 211
PV 28,124,228
E 212
SM 224,224
E 214
SM 224,228
C 233,210
s_waitcnt lgkmcnt(0)
s_barrier
QZ 208,0,176
E 213
QK 208,4,180
E 215
QK 208,8,184
E 216
QK 208,12,188
C 234,212
C 235,214
s_waitcnt vmcnt(14)
v_lshlrev_b32 v176, 16, v128
QZ 212,16,176
E 218
QK 212,20,180
v_and_b32 v128, s83, v128
KR 0,241,16384
QK 212,24,184
E 217
E 219
v_mul_f32 v176, s82, v176
QK 212,28,188
C 236,216
C 237,218
QZ 216,32,176
v_mul_f32 v128, s82, v128
s_mov_b32 s60, .LSTEP6-.LCODE0
QK 216,36,180
E 220
E 222
v_cvt_pk_bf16_f32 v128, v176, v128
QK 216,40,184
E 221
E 223
QK 216,44,188
C 238,220
C 239,222
v_lshlrev_b32 v177, 16, v129
QZ 220,48,176
v_and_b32 v129, s83, v129
KR 4,241,17408
QK 220,52,180
v_mul_f32 v177, s82, v177
v_mul_f32 v129, s82, v129
QK 220,56,184
KR 8,241,18432
v_cvt_pk_bf16_f32 v129, v177, v129
QK 220,60,188
v_lshlrev_b32 v178, 16, v130
v_and_b32 v130, s83, v130
PV 32,64,232
KR 12,241,19456
v_mul_f32 v178, s82, v178
PV 32,96,236
v_mul_f32 v130, s82, v130
v_cvt_pk_bf16_f32 v130, v178, v130
PV 36,68,232
KR 16,241,20480
v_lshlrev_b32 v179, 16, v131
PV 36,100,236
E 192
PV 40,72,232
KR 20,241,21504
v_and_b32 v131, s83, v131
PV 40,104,236
E 194
PV 44,76,232
KR 24,241,22528
v_mul_f32 v179, s82, v179
PV 44,108,236
E 193
PV 48,80,232
KR 28,241,23552
C 224,192
PV 48,112,236
E 195
PV 52,84,232
buffer_load_dword a244, v252, s[56:59], s60 offen
PV 52,116,236
KR 32,241,24576
C 225,194
PV 56,88,232
E 196
PV 56,120,236
KR 36,241,25600
s_add_u32 s86, s81, 69632
PV 60,92,232
E 198
PV 60,124,236
v_mul_f32 v131, s82, v131
s_waitcnt lgkmcnt(4)
SM 228,232
v_cvt_pk_bf16_f32 v131, v179, v131
KR 40,241,26624
SM 228,236
buffer_load_dwordx4 a[208:211], v246, s[64:67], s86 offen offset:0
QZ 192,0,128
v_lshlrev_b32 v180, 16, v132
s_waitcnt lgkmcnt(3)
QK 192,4,132
v_and_b32 v132, s83, v132
KR 44,241,27648
QK 192,8,136
E 197
E 199
QK 192,12,140
C 226,196
C 227,198
v_mul_f32 v180, s82, v180
QZ 196,16,128
KR 48,241,28672
v_mul_f32 v132, s82, v132
QK 196,20,132
E 200
E 202
KR 52,241,29696
QK 196,24,136
E 201
E 203
KR 56,241,30720
v_cvt_pk_bf16_f32 v132, v180, v132
QK 196,28,140
C 228,200
C 229,202
KR 60,241,31744
s_waitcnt lgkmcnt(4)
v_lshlrev_b32 v181, 16, v133
QZ 200,32,128
buffer_load_dwordx4 a[212:215], v246, s[64:67], s86 offen offset:64
QK 200,36,132
E 204
E 206
v_and_b32 v133, s83, v133
QK 200,40,136
E 205
E 207
QK 200,44,140
C 230,204
C 231,206
s_waitcnt lgkmcnt(0)
v_mul_f32 v181, s82, v181
QZ 204,48,128
v_mul_f32 v133, s82, v133
v_cvt_pk_bf16_f32 v133, v181, v133
QK 204,52,132
v_lshlrev_b32 v182, 16, v134
v_and_b32 v134, s83, v134
QK 204,56,136
buffer_load_dwordx4 a[216:219], v246, s[64:67], s86 offen offset:128
QK 204,60,140
v_mul_f32 v182, s82, v182
v_mul_f32 v134, s82, v134
PV 64,64,224
v_cvt_pk_bf16_f32 v134, v182, v134
v_lshlrev_b32 v183, 16, v135
PV 64,96,228
v_and_b32 v135, s83, v135
v_mul_f32 v183, s82, v183
PV 68,68,224
v_mul_f32 v135, s82, v135
v_cvt_pk_bf16_f32 v135, v183, v135
PV 68,100,228
E 208
PV 72,72,224
buffer_load_dwordx4 a[220:223], v246, s[64:67], s86 offen offset:192
PV 72,104,228
E 210
PV 76,76,224
E 209
PV 76,108,228
E 211
PV 80,80,224
E 212
PV 80,112,228
E 214
PV 84,84,224
E 213
PV 84,116,228
E 215
PV 88,88,224
E 216
PV 88,120,228
E 218
PV 92,92,224
E 217
PV 92,124,228
v_lshlrev_b32 v176, 16, v136
C 232,208
SM 232,224
v_and_b32 v136, s83, v136
C 233,210
SM 232,228
E 219
.LSTEP5:
QZ 208,0,144
v_mul_f32 v176, s82, v176
C 234,212
QK 208,4,148
v_mul_f32 v136, s82, v136
C 235,214
QK 208,8,152
E 220
QK 208,12,156
v_cvt_pk_bf16_f32 v136, v176, v136
C 236,216
QZ 212,16,144
v_lshlrev_b32 v177, 16, v137
C 237,218
QK 212,20,148
E 222
QK 212,24,152
v_and_b32 v137, s83, v137
v_mul_f32 v177, s82, v177
QK 212,28,156
E 221
QZ 216,32,144
E 223
QK 216,36,148
v_mul_f32 v137, s82, v137
C 238,220
QK 216,40,152
v_cvt_pk_bf16_f32 v137, v177, v137
C 239,222
QK 216,44,156
v_lshlrev_b32 v178, 16, v138
v_and_b32 v138, s83, v138
QZ 220,48,144
v_mul_f32 v178, s82, v178
v_mul_f32 v138, s82, v138
QK 220,52,148
v_cvt_pk_bf16_f32 v138, v178, v138
v_lshlrev_b32 v179, 16, v139
QK 220,56,152
v_and_b32 v139, s83, v139
v_mul_f32 v179, s82, v179
QK 220,60,156
v_mul_f32 v139, s82, v139
v_cvt_pk_bf16_f32 v139, v179, v139
PV 96,64,232
v_lshlrev_b32 v180, 16, v140
v_and_b32 v140, s83, v140
PV 96,96,236
VR 64,188,49152
VR 66,188,53248
PV 100,68,232
VR 96,188,57344
v_mul_f32 v180, s82, v180
PV 100,100,236
VR 98,188,61440
v_mul_f32 v140, s82, v140
PV 104,72,232
VR 68,189,49152
v_cvt_pk_bf16_f32 v140, v180, v140
PV 104,104,236
VR 70,189,53248
v_lshlrev_b32 v181, 16, v141
PV 108,76,232
VR 100,189,57344
v_and_b32 v141, s83, v141
PV 108,108,236
VR 102,189,61440
v_mul_f32 v181, s82, v181
PV 112,80,232
VR 72,188,49408
v_mul_f32 v141, s82, v141
PV 112,112,236
VR 74,188,53504
v_cvt_pk_bf16_f32 v141, v181, v141
PV 116,84,232
VR 104,188,57600
v_lshlrev_b32 v182, 16, v142
PV 116,116,236
VR 106,188,61696
VR 76,189,49408
PV 120,88,232
VR 78,189,53504
v_and_b32 v142, s83, v142
PV 120,120,236
VR 108,189,57600
v_mul_f32 v182, s82, v182
PV 124,92,232
VR 110,189,61696
v_mul_f32 v142, s82, v142
PV 124,124,236
E 192
E 194
SM 236,232
E 193
E 195
SM 236,236
C 224,192
C 225,194
QZ 192,0,160
VR 80,188,49664
VR 82,188,53760
QK 192,4,164
E 196
E 198
QK 192,8,168
E 197
E 199
QK 192,12,172
C 226,196
C 227,198
QZ 196,16,160
VR 112,188,57856
VR 114,188,61952
QK 196,20,164
E 200
E 202
QK 196,24,168
E 201
E 203
QK 196,28,172
C 228,200
C 229,202
QZ 200,32,160
VR 84,189,49664
VR 86,189,53760
QK 200,36,164
E 204
E 206
QK 200,40,168
E 205
E 207
QK 200,44,172
C 230,204
C 231,206
QZ 204,48,160
VR 116,189,57856
s_waitcnt lgkmcnt(3)
QK 204,52,164
VR 118,189,61952
VR 88,188,49920
QK 204,56,168
VR 90,188,54016
v_cvt_pk_bf16_f32 v142, v182, v142
QK 204,60,172
VR 120,188,58112
v_lshlrev_b32 v183, 16, v143
PV 0,64,224
VR 122,188,62208
v_and_b32 v143, s83, v143
PV 0,96,228
VR 92,189,49920
v_mul_f32 v183, s82, v183
PV 4,68,224
VR 94,189,54016
v_mul_f32 v143, s82, v143
PV 4,100,228
VR 124,189,58112
v_cvt_pk_bf16_f32 v143, v183, v143
PV 8,72,224
VR 126,189,62208
PV 8,104,228
E 208
PV 12,76,224
E 210
PV 12,108,228
E 209
PV 16,80,224
ds_write_b128 v184, v[128:131] offset:32768
PV 16,112,228
s_waitcnt lgkmcnt(6)
ds_write_b128 v185, v[132:135] offset:33024
PV 20,84,224
ds_write_b128 v186, v[136:139] offset:33280
PV 20,116,228
s_waitcnt lgkmcnt(6)
ds_write_b128 v187, v[140:143] offset:33536
PV 24,88,224
E 211
PV 24,120,228
s_waitcnt lgkmcnt(4)
C 232,208
PV 28,92,224
E 212
PV 28,124,228
E 214
SM 224,224
E 213
SM 224,228
C 233,210
s_waitcnt lgkmcnt(0)
s_barrier
QZ 208,0,176
E 215
QK 208,4,180
E 216
QK 208,8,184
E 218
QK 208,12,188
C 234,212
C 235,214
s_waitcnt vmcnt(10)
v_lshlrev_b32 v176, 16, v144
QZ 212,16,176
E 217
QK 212,20,180
v_and_b32 v144, s83, v144
C 236,216
QK 212,24,184
E 219
v_mul_f32 v176, s82, v176
QK 212,28,188
C 237,218
KR 0,241,32768
QZ 216,32,176
v_mul_f32 v144, s82, v144
s_mov_b32 s60, .LSTEP7-.LCODE0
QK 216,36,180
E 220
E 222
v_cvt_pk_bf16_f32 v144, v176, v144
QK 216,40,184
E 221
E 223
QK 216,44,188
C 238,220
C 239,222
v_lshlrev_b32 v177, 16, v145
QZ 220,48,176
v_and_b32 v145, s83, v145
KR 4,241,33792
QK 220,52,180
v_mul_f32 v177, s82, v177
v_mul_f32 v145, s82, v145
QK 220,56,184
KR 8,241,34816
v_cvt_pk_bf16_f32 v145, v177, v145
QK 220,60,188
v_lshlrev_b32 v178, 16, v146
v_and_b32 v146, s83, v146
PV 32,64,232
KR 12,241,35840
v_mul_f32 v178, s82, v178
PV 32,96,236
v_mul_f32 v146, s82, v146
v_cvt_pk_bf16_f32 v146, v178, v146
PV 36,68,232
KR 16,241,36864
v_lshlrev_b32 v179, 16, v147
PV 36,100,236
E 192
PV 40,72,232
KR 20,241,37888
v_lshlrev_b32 v255, 3, v255
PV 40,104,236
E 194
PV 44,76,232
KR 24,241,38912
v_and_b32 v147, s83, v147
PV 44,108,236
E 193
PV 48,80,232
KR 28,241,39936
C 224,192
PV 48,112,236
E 195
PV 52,84,232
buffer_load_dword a244, v252, s[56:59], s60 offen
PV 52,116,236
KR 32,241,40960
C 225,194
PV 56,88,232
E 196
PV 56,120,236
v_mul_f32 v179, s82, v179
s_add_u32 s86, s81, 73728
PV 60,92,232
KR 36,241,41984
s_waitcnt lgkmcnt(4)
PV 60,124,236
v_mul_f32 v147, s82, v147
v_cvt_pk_bf16_f32 v147, v179, v147
SM 228,232
KR 40,241,43008
v_lshlrev_b32 v180, 16, v148
SM 228,236
buffer_load_dword a246, v255, s[64:67], s86 offen
QZ 192,0,128
KR 44,241,44032
s_waitcnt lgkmcnt(4)
QK 192,4,132
E 198
v_and_b32 v148, s83, v148
QK 192,8,136
E 197
E 199
QK 192,12,140
C 226,196
C 227,198
v_mul_f32 v180, s82, v180
QZ 196,16,128
KR 48,241,45056
v_mul_f32 v148, s82, v148
QK 196,20,132
E 200
E 202
KR 52,241,46080
QK 196,24,136
E 201
E 203
KR 56,241,47104
v_cvt_pk_bf16_f32 v148, v180, v148
QK 196,28,140
C 228,200
C 229,202
KR 60,241,48128
s_waitcnt lgkmcnt(4)
v_lshlrev_b32 v181, 16, v149
QZ 200,32,128
E 204
QK 200,36,132
E 206
v_and_b32 v149, s83, v149
QK 200,40,136
E 205
E 207
QK 200,44,140
C 230,204
C 231,206
s_waitcnt lgkmcnt(0)
v_mul_f32 v181, s82, v181
QZ 204,48,128
v_mul_f32 v149, s82, v149
v_cvt_pk_bf16_f32 v149, v181, v149
QK 204,52,132
v_lshlrev_b32 v182, 16, v150
v_and_b32 v150, s83, v150
QK 204,56,136
v_mul_f32 v182, s82, v182
v_mul_f32 v150, s82, v150
QK 204,60,140
v_cvt_pk_bf16_f32 v150, v182, v150
v_lshlrev_b32 v183, 16, v151
PV 64,64,224
v_and_b32 v151, s83, v151
v_mul_f32 v183, s82, v183
PV 64,96,228
v_mul_f32 v151, s82, v151
v_cvt_pk_bf16_f32 v151, v183, v151
PV 68,68,224
E 208
PV 68,100,228
E 210
PV 72,72,224
E 209
PV 72,104,228
E 211
PV 76,76,224
C 232,208
C 233,210
PV 76,108,228
E 212
PV 80,80,224
E 214
PV 80,112,228
E 213
PV 84,84,224
E 215
PV 84,116,228
E 216
PV 88,88,224
E 218
PV 88,120,228
E 217
PV 92,92,224
E 219
PV 92,124,228
v_lshlrev_b32 v176, 16, v152
C 234,212
SM 232,224
v_and_b32 v152, s83, v152
C 235,214
SM 232,228
E 220
.LSTEP6:
QZ 208,0,144
v_mul_f32 v176, s82, v176
C 236,216
QK 208,4,148
v_mul_f32 v152, s82, v152
C 237,218
QK 208,8,152
E 222
QK 208,12,156
v_cvt_pk_bf16_f32 v152, v176, v152
v_lshlrev_b32 v177, 16, v153
QZ 212,16,144
E 221
QK 212,20,148
E 223
QK 212,24,152
v_and_b32 v153, s83, v153
C 238,220
QK 212,28,156
v_mul_f32 v177, s82, v177
C 239,222
QZ 216,32,144
v_mul_f32 v153, s82, v153
v_cvt_pk_bf16_f32 v153, v177, v153
QK 216,36,148
v_lshlrev_b32 v178, 16, v154
v_and_b32 v154, s83, v154
QK 216,40,152
v_mul_f32 v178, s82, v178
v_mul_f32 v154, s82, v154
QK 216,44,156
v_cvt_pk_bf16_f32 v154, v178, v154
v_lshlrev_b32 v179, 16, v155
QZ 220,48,144
v_and_b32 v155, s83, v155
v_mul_f32 v179, s82, v179
QK 220,52,148
v_mul_f32 v155, s82, v155
v_cvt_pk_bf16_f32 v155, v179, v155
QK 220,56,152
v_lshlrev_b32 v180, 16, v156
v_and_b32 v156, s83, v156
QK 220,60,156
v_mul_f32 v180, s82, v180
v_mul_f32 v156, s82, v156
PV 96,64,232
E 192
PV 96,96,236
VR 64,242,0
VR 66,242,4096
PV 100,68,232
VR 96,242,8192
v_cvt_pk_bf16_f32 v156, v180, v156
PV 100,100,236
VR 98,242,12288
v_lshlrev_b32 v181, 16, v157
PV 104,72,232
VR 68,243,0
v_and_b32 v157, s83, v157
PV 104,104,236
VR 70,243,4096
v_mul_f32 v181, s82, v181
PV 108,76,232
VR 100,243,8192
v_mul_f32 v157, s82, v157
PV 108,108,236
VR 102,243,12288
v_cvt_pk_bf16_f32 v157, v181, v157
PV 112,80,232
VR 72,242,256
v_lshlrev_b32 v182, 16, v158
PV 112,112,236
VR 74,242,4352
v_and_b32 v158, s83, v158
PV 116,84,232
VR 104,242,8448
v_mul_f32 v182, s82, v182
PV 116,116,236
VR 106,242,12544
VR 76,243,256
PV 120,88,232
VR 78,243,4352
v_mul_f32 v158, s82, v158
PV 120,120,236
VR 108,243,8448
v_cvt_pk_bf16_f32 v158, v182, v158
PV 124,92,232
VR 110,243,12544
v_lshlrev_b32 v183, 16, v159
PV 124,124,236
E 194
SM 236,232
E 193
E 195
SM 236,236
C 224,192
C 225,194
QZ 192,0,160
VR 80,242,512
VR 82,242,4608
QK 192,4,164
E 196
E 198
QK 192,8,168
E 197
E 199
QK 192,12,172
C 226,196
C 227,198
QZ 196,16,160
VR 112,242,8704
VR 114,242,12800
QK 196,20,164
E 200
E 202
QK 196,24,168
E 201
E 203
QK 196,28,172
C 228,200
C 229,202
QZ 200,32,160
VR 84,243,512
VR 86,243,4608
QK 200,36,164
E 204
E 206
QK 200,40,168
E 205
E 207
QK 200,44,172
C 230,204
C 231,206
QZ 204,48,160
VR 116,243,8704
s_waitcnt lgkmcnt(3)
QK 204,52,164
VR 118,243,12800
VR 88,242,768
QK 204,56,168
VR 90,242,4864
v_and_b32 v159, s83, v159
QK 204,60,172
VR 120,242,8960
v_mul_f32 v183, s82, v183
PV 0,64,224
VR 122,242,13056
v_mul_f32 v159, s82, v159
PV 0,96,228
VR 92,243,768
v_cvt_pk_bf16_f32 v159, v183, v159
PV 4,68,224
VR 94,243,4864
PV 4,100,228
VR 124,243,8960
PV 8,72,224
VR 126,243,13056
PV 8,104,228
E 208
PV 12,76,224
E 210
PV 12,108,228
E 209
PV 16,80,224
ds_write_b128 v184, v[144:147] offset:49152
PV 16,112,228
s_waitcnt lgkmcnt(7)
ds_write_b128 v185, v[148:151] offset:49408
PV 20,84,224
ds_write_b128 v186, v[152:155] offset:49664
PV 20,116,228
s_waitcnt lgkmcnt(7)
ds_write_b128 v187, v[156:159] offset:49920
PV 24,88,224
E 211
PV 24,120,228
s_waitcnt lgkmcnt(4)
C 232,208
PV 28,92,224
E 212
PV 28,124,228
E 214
SM 224,224
E 213
SM 224,228
C 233,210
s_waitcnt lgkmcnt(0)
s_barrier
s_add_u32 s84, s80, 0
s_add_u32 m0, s80, 131072
QZ 208,0,176
buffer_load_dwordx4 v244, s[72:75], s84 offen offset:0  lds
QK 208,4,180
E 215
QK 208,8,184
E 216
QK 208,12,188
C 234,212
C 235,214
QZ 212,16,176
E 218
QK 212,20,180
KR 0,241,49152
s_mov_b32 s60, .LSTEP8-.LCODE0
QK 212,24,184
E 217
E 219
QK 212,28,188
C 236,216
C 237,218
buffer_load_dwordx4 v245, s[72:75], s84 offen offset:1024  lds
QZ 216,32,176
KR 4,241,50176
QK 216,36,180
E 220
E 222
QK 216,40,184
E 221
E 223
QK 216,44,188
C 238,220
C 239,222
QZ 220,48,176
KR 8,241,51200
QK 220,52,180
buffer_load_dwordx4 v244, s[72:75], s84 offen offset:2048  lds
QK 220,56,184
KR 12,241,52224
QK 220,60,188
E 192
PV 32,64,232
KR 16,241,53248
PV 32,96,236
E 194
PV 36,68,232
KR 20,241,54272
PV 36,100,236
buffer_load_dwordx4 v245, s[72:75], s84 offen offset:3072  lds
PV 40,72,232
KR 24,241,55296
PV 40,104,236
E 193
PV 44,76,232
KR 28,241,56320
C 224,192
PV 44,108,236
E 195
PV 48,80,232
KR 32,241,57344
C 225,194
PV 48,112,236
E 196
PV 52,84,232
buffer_load_dword a244, v252, s[56:59], s60 offen
PV 52,116,236
KR 36,241,58368
PV 56,88,232
E 198
PV 56,120,236
KR 40,241,59392
PV 60,92,232
E 197
PV 60,124,236
KR 44,241,60416
s_waitcnt lgkmcnt(5)
SM 228,232
E 199
SM 228,236
KR 48,241,61440
C 226,196
QZ 192,0,128
E 200
QK 192,4,132
KR 52,241,62464
C 227,198
QK 192,8,136
E 202
QK 192,12,140
s_waitcnt lgkmcnt(5)
KR 56,241,63488
QZ 196,16,128
E 201
QK 196,20,132
KR 60,241,64512
C 228,200
QK 196,24,136
E 203
QK 196,28,140
C 229,202
s_waitcnt lgkmcnt(4)
QZ 200,32,128
E 204
QK 200,36,132
E 206
QK 200,40,136
E 205
E 207
QK 200,44,140
C 230,204
C 231,206
s_waitcnt lgkmcnt(0)
QZ 204,48,128
E 208
QK 204,52,132
E 210
QK 204,56,136
E 209
QK 204,60,140
s_add_u32 s86, s81, 73728
C 232,208
PV 64,64,224
buffer_load_dwordx4 a[128:131], v246, s[64:67], s86 offen offset:0
PV 64,96,228
E 211
PV 68,68,224
E 212
PV 68,100,228
E 214
PV 72,72,224
E 213
PV 72,104,228
C 233,210
C 234,212
PV 76,76,224
buffer_load_dwordx4 a[132:135], v246, s[64:67], s86 offen offset:64
PV 76,108,228
E 215
PV 80,80,224
E 216
PV 80,112,228
E 218
PV 84,84,224
E 217
PV 84,116,228
C 235,214
C 236,216
PV 88,88,224
E 219
PV 88,120,228
buffer_load_dwordx4 a[136:139], v246, s[64:67], s86 offen offset:128
PV 92,92,224
E 220
PV 92,124,228
E 222
SM 232,224
E 221
SM 232,228
E 223
.LSTEP7:
QZ 208,0,144
C 237,218
C 238,220
QK 208,4,148
E 192
QK 208,8,152
buffer_load_dwordx4 a[140:143], v246, s[64:67], s86 offen offset:192
QK 208,12,156
E 194
QZ 212,16,144
E 193
QK 212,20,148
C 239,222
C 224,192
QK 212,24,152
E 195
QK 212,28,156
E 196
QZ 216,32,144
E 198
QK 216,36,148
E 197
QK 216,40,152
C 225,194
C 226,196
QK 216,44,156
E 199
QZ 220,48,144
s_add_u32 s86, s81, 77824
C 227,198
QK 220,52,148
E 200
QK 220,56,152
E 202
QK 220,60,156
E 201
PV 96,64,232
buffer_load_dwordx4 a[144:147], v246, s[64:67], s86 offen offset:0
PV 96,96,236
VR 64,242,16384
C 228,200
PV 100,68,232
VR 66,242,20480
VR 96,242,24576
PV 100,100,236
VR 98,242,28672
PV 104,72,232
VR 68,243,16384
PV 104,104,236
VR 70,243,20480
PV 108,76,232
buffer_load_dwordx4 a[148:151], v246, s[64:67], s86 offen offset:64
PV 108,108,236
VR 100,243,24576
VR 102,243,28672
PV 112,80,232
VR 72,242,16640
PV 112,112,236
VR 74,242,20736
PV 116,84,232
VR 104,242,24832
PV 116,116,236
VR 106,242,28928
PV 120,88,232
VR 76,243,16640
PV 120,120,236
buffer_load_dwordx4 a[152:155], v246, s[64:67], s86 offen offset:128
PV 124,92,232
VR 78,243,20736
VR 108,243,24832
PV 124,124,236
VR 110,243,28928
SM 236,232
VR 80,242,16896
SM 236,236
VR 82,242,20992
QZ 192,0,160
VR 112,242,25088
QK 192,4,164
VR 114,242,29184
QK 192,8,168
buffer_load_dwordx4 a[156:159], v246, s[64:67], s86 offen offset:192
QK 192,12,172
VR 84,243,16896
VR 86,243,20992
QZ 196,16,160
VR 116,243,25088
QK 196,20,164
VR 118,243,29184
QK 196,24,168
E 203
QK 196,28,172
C 229,202
VR 88,242,17152
QZ 200,32,160
VR 90,242,21248
VR 120,242,25344
QK 200,36,164
E 204
E 206
QK 200,40,168
E 205
E 207
QK 200,44,172
C 230,204
C 231,206
QZ 204,48,160
VR 122,242,29440
s_waitcnt lgkmcnt(6)
QK 204,52,164
VR 92,243,17152
VR 94,243,21248
QK 204,56,168
VR 124,243,25344
QK 204,60,172
VR 126,243,29440
PV 0,64,224
E 208
PV 0,96,228
E 210
PV 4,68,224
E 209
PV 4,100,228
E 211
PV 8,72,224
C 232,208
C 233,210
PV 8,104,228
E 212
PV 12,76,224
E 214
PV 12,108,228
E 213
PV 16,80,224
E 215
PV 16,112,228
s_waitcnt lgkmcnt(2)
C 234,212
PV 20,84,224
s_waitcnt lgkmcnt(0)
C 235,214
PV 20,116,228
E 216
PV 24,88,224
E 218
PV 24,120,228
E 217
PV 28,92,224
E 219
PV 28,124,228
E 220
SM 224,224
E 222
SM 224,228
s_waitcnt vmcnt(9)
s_barrier
s_add_u32 s84, s80, 16384
s_add_u32 m0, s80, 147456
QZ 208,0,176
buffer_load_dwordx4 v244, s[72:75], s84 offen offset:0  lds
QK 208,4,180
KR 0,240,0
C 236,216
QK 208,8,184
E 221
QK 208,12,188
KR 4,240,1024
C 237,218
QZ 212,16,176
E 223
QK 212,20,180
KR 8,240,2048
C 238,220
QK 212,24,184
buffer_load_dwordx4 v245, s[72:75], s84 offen offset:1024  lds
QK 212,28,188
KR 12,240,3072
C 239,222
QZ 216,32,176
v_accvgpr_read_b32 v248, a224
v_subrev_u32 v253, s88, v248
QK 216,36,180
KR 16,240,4096
s_mov_b32 s60, .LSTEP9-.LCODE0
QK 216,40,184
v_rcp_f32 v248, v248
QK 216,44,188
MU 0,248
KR 20,240,5120
QZ 220,48,176
buffer_load_dwordx4 v244, s[72:75], s84 offen offset:2048  lds
QK 220,52,180
KR 24,240,6144
v_max_u32 v254, v254, v253
QK 220,56,184
s_add_u32 s96, s81, 0
MU 8,248
QK 220,60,188
KR 28,240,7168
MU 16,248
PV 32,64,232
MU 24,248
MU 1,248
PV 32,96,236
KR 32,240,8192
MU 2,248
PV 36,68,232
MU 9,248
MU 17,248
PV 36,100,236
buffer_load_dwordx4 v245, s[72:75], s84 offen offset:3072  lds
PV 40,72,232
KR 36,240,9216
MU 25,248
PV 40,104,236
E 192
PV 44,76,232
KR 40,240,10240
MU 3,248
PV 44,108,236
E 194
PV 48,80,232
KR 44,240,11264
MU 10,248
PV 48,112,236
E 193
PV 52,84,232
buffer_load_dword a244, v252, s[56:59], s60 offen
PV 52,116,236
KR 48,240,12288
C 224,192
PV 56,88,232
E 195
PV 56,120,236
KR 52,240,13312
C 225,194
PV 60,92,232
E 196
PV 60,124,236
KR 56,240,14336
s_waitcnt lgkmcnt(5)
SM 228,232
E 198
SM 228,236
KR 60,240,15360
MU 4,248
QZ 192,0,192
E 197
QK 192,4,196
E 199
QK 192,8,200
E 200
QK 192,12,204
C 226,196
C 227,198
QZ 196,16,192
E 202
QK 196,20,196
E 201
QK 196,24,200
E 203
QK 196,28,204
C 228,200
C 229,202
s_waitcnt lgkmcnt(1)
QZ 200,32,192
E 204
QK 200,36,196
E 206
MU 18,248
MU 26,248
QK 200,40,200
E 205
E 207
QK 200,44,204
C 230,204
C 231,206
s_waitcnt lgkmcnt(0)
v_accvgpr_read_b32 v249, a228
v_subrev_u32 v253, s88, v249
v_rcp_f32 v249, v249
s_nop 0
MU 32,249
QZ 204,48,192
v_max_u32 v254, v254, v253
s_add_u32 s97, s81, 4096
QK 204,52,196
MU 40,249
MU 48,249
QK 204,56,200
MU 56,249
MU 5,248
QK 204,60,204
MU 33,249
MU 11,248
PV 64,64,224
MU 19,248
MU 27,248
PV 64,96,228
MU 6,248
MU 34,249
PV 68,68,224
MU 41,249
MU 49,249
PV 68,100,228
E 208
PV 72,72,224
E 210
PV 72,104,228
E 209
PV 76,76,224
E 211
PV 76,108,228
E 212
PV 80,80,224
E 214
PV 80,112,228
E 213
PV 84,84,224
E 215
PV 84,116,228
E 216
PV 88,88,224
E 218
PV 88,120,228
E 217
PV 92,92,224
MU 57,249
C 232,208
PV 92,124,228
E 219
SM 232,224
E 220
SM 232,228
C 233,210
C 234,212
.LSTEP8:
QZ 208,0,208
MU 7,248
MU 12,248
QK 208,4,212
MU 20,248
MU 35,249
QK 208,8,216
E 222
QK 208,12,220
C 235,214
MU 28,248
QZ 212,16,208
E 221
QK 212,20,212
E 223
QK 212,24,216
C 236,216
C 237,218
QK 212,28,220
C 238,220
C 239,222
QZ 216,32,208
C 0,0
MU 42,249
QK 216,36,212
MU 36,249
MU 50,249
MU 58,249
QK 216,40,216
v_accvgpr_read_b32 v250, a232
v_subrev_u32 v253, s88, v250
QK 216,44,220
v_max_u32 v254, v254, v253
s_add_u32 s98, s81, 8192
QZ 220,48,208
MU 13,248
MU 21,248
QK 220,52,212
C 1,2
MU 29,248
QK 220,56,216
MU 37,249
MU 43,249
QK 220,60,220
MU 51,249
MU 59,249
PV 96,64,232
C 2,4
MU 14,248
PV 96,96,236
VR 64,242,0
VR 66,242,4096
PV 100,68,232
VR 96,242,8192
MU 38,249
PV 100,100,236
VR 98,242,12288
MU 22,248
PV 104,72,232
VR 68,243,0
MU 30,248
PV 104,104,236
VR 70,243,4096
C 3,6
PV 108,76,232
VR 100,243,8192
MU 39,249
PV 108,108,236
VR 102,243,12288
MU 44,249
PV 112,80,232
VR 72,242,256
MU 52,249
PV 112,112,236
VR 74,242,4352
MU 60,249
PV 116,84,232
VR 104,242,8448
MU 15,248
PV 116,116,236
VR 106,242,12544
VR 76,243,256
PV 120,88,232
VR 78,243,4352
v_permlane16_swap_b32 v0, v2
PV 120,120,236
VR 108,243,8448
MU 23,248
PV 124,92,232
VR 110,243,12544
C 32,32
PV 124,124,236
E 192
E 194
SM 236,232
E 193
E 195
SM 236,236
C 224,192
C 225,194
s_waitcnt vmcnt(9)
QZ 192,0,128
VR 80,242,512
VR 82,242,4608
QK 192,4,132
E 196
E 198
QK 192,8,136
E 197
E 199
QK 192,12,140
C 226,196
C 227,198
QZ 196,16,128
VR 112,242,8704
VR 114,242,12800
QK 196,20,132
E 200
E 202
QK 196,24,136
E 201
E 203
QK 196,28,140
C 228,200
C 229,202
QZ 200,32,128
VR 84,243,512
VR 86,243,4608
QK 200,36,132
E 204
E 206
QK 200,40,136
E 205
E 207
MU 31,248
QK 200,44,140
C 230,204
C 231,206
QZ 204,48,128
VR 116,243,8704
s_waitcnt lgkmcnt(3)
QK 204,52,132
MU 45,249
VR 118,243,12800
QK 204,56,136
VR 88,242,768
VR 90,242,4864
QK 204,60,140
v_permlane16_swap_b32 v1, v3
MU 53,249
PZ 128,64,224
C 33,34
MU 61,249
PV 128,96,228
buffer_store_dwordx4 v[0:3], v247, s[76:79], s96 offen offset:0 sc0 sc1
PZ 132,68,224
C 8,8
VR 120,242,8960
PV 132,100,228
VR 122,242,13056
VR 92,243,768
PZ 136,72,224
VR 94,243,4864
v_accvgpr_read_b32 v251, a236
PV 136,104,228
C 16,16
VR 124,243,8960
PZ 140,76,224
VR 126,243,13056
s_waitcnt lgkmcnt(7)
PV 140,108,228
C 24,24
v_subrev_u32 v253, s88, v251
PZ 144,80,224
C 34,36
s_waitcnt lgkmcnt(4)
PV 144,112,228
E 208
PZ 148,84,224
MU 46,249
s_waitcnt lgkmcnt(0)
PV 148,116,228
E 210
PZ 152,88,224
MU 54,249
v_max_u32 v254, v254, v253
PV 152,120,228
E 209
PZ 156,92,224
MU 62,249
C 232,208
PV 156,124,228
C 9,10
s_add_u32 s84, s80, 32768
SZ 224,224
E 211
C 35,38
SM 224,228
C 233,210
s_waitcnt vmcnt(6)
s_waitcnt vmcnt(2)
s_barrier
s_add_u32 m0, s80, 131072
QZ 208,0,144
buffer_load_dwordx4 v244, s[72:75], s84 offen offset:0  lds
C 17,18
QK 208,4,148
E 212
E 214
QK 208,8,152
E 213
E 215
QK 208,12,156
C 234,212
C 235,214
C 25,26
QZ 212,16,144
KR 0,240,16384
s_mov_b32 s60, .LSTEP10-.LCODE0
QK 212,20,148
E 216
E 218
MU 47,249
QK 212,24,152
E 217
E 219
v_permlane16_swap_b32 v32, v34
QK 212,28,156
C 236,216
C 237,218
buffer_load_dwordx4 v245, s[72:75], s84 offen offset:1024  lds
MU 55,249
QZ 216,32,144
KR 4,240,17408
s_add_u32 s99, s81, 12288
QK 216,36,148
E 220
E 222
QK 216,40,152
E 221
E 223
C 10,12
MU 63,249
QK 216,44,156
C 238,220
C 239,222
QZ 220,48,144
KR 8,240,18432
C 18,20
QK 220,52,148
buffer_load_dwordx4 v244, s[72:75], s84 offen offset:2048  lds
QK 220,56,152
KR 12,240,19456
v_permlane16_swap_b32 v33, v35
QK 220,60,156
C 26,28
C 40,40
PZ 160,64,232
KR 16,240,20480
C 48,48
PV 160,96,236
buffer_store_dwordx4 v[32:35], v247, s[76:79], s97 offen offset:0 sc0 sc1
PZ 164,68,232
KR 20,240,21504
C 11,14
PV 164,100,236
E 192
PZ 168,72,232
KR 24,240,22528
C 56,56
PV 168,104,236
buffer_load_dwordx4 v245, s[72:75], s84 offen offset:3072  lds
PZ 172,76,232
KR 28,240,23552
C 19,22
PV 172,108,236
E 194
PZ 176,80,232
KR 32,240,24576
C 27,30
PV 176,112,236
E 193
PZ 180,84,232
KR 36,240,25600
C 224,192
PV 180,116,236
buffer_load_dword a244, v252, s[56:59], s60 offen
PZ 184,88,232
KR 40,240,26624
C 41,42
PV 184,120,236
E 195
PZ 188,92,232
KR 44,240,27648
C 225,194
PV 188,124,236
E 196
SZ 228,232
KR 48,240,28672
s_waitcnt lgkmcnt(5)
SM 228,236
E 198
QZ 192,0,192
C 49,50
KR 52,240,29696
QK 192,4,196
E 197
QK 192,8,200
E 199
v_permlane16_swap_b32 v8, v10
QK 192,12,204
C 226,196
C 227,198
C 57,58
QZ 196,16,192
KR 56,240,30720
s_waitcnt lgkmcnt(3)
QK 196,20,196
E 200
E 202
QK 196,24,200
E 201
E 203
v_permlane16_swap_b32 v16, v18
QK 196,28,204
C 228,200
C 229,202
KR 60,240,31744
QZ 200,32,192
E 204
QK 200,36,196
E 206
v_rcp_f32 v250, v250
s_nop 0
MU 64,250
QK 200,40,200
E 205
E 207
v_permlane16_swap_b32 v24, v26
C 42,44
QK 200,44,204
C 230,204
C 231,206
s_waitcnt lgkmcnt(0)
QZ 204,48,192
C 50,52
v_permlane16_swap_b32 v9, v11
QK 204,52,196
MU 72,250
C 58,60
QK 204,56,200
MU 65,250
v_permlane16_swap_b32 v17, v19
QK 204,60,204
C 43,46
MU 80,250
PZ 0,64,224
buffer_store_dwordx4 v[8:11], v247, s[76:79], s96 offen offset:64 sc0 sc1
PV 0,96,228
v_permlane16_swap_b32 v25, v27
MU 66,250
PZ 4,68,224
C 51,54
C 59,62
PV 4,100,228
E 208
PZ 8,72,224
E 210
PV 8,104,228
E 209
PZ 12,76,224
buffer_store_dwordx4 v[16:19], v247, s[76:79], s96 offen offset:128 sc0 sc1
PV 12,108,228
E 211
PZ 16,80,224
E 212
PV 16,112,228
E 214
PZ 20,84,224
buffer_store_dwordx4 v[24:27], v247, s[76:79], s96 offen offset:192 sc0 sc1
PV 20,116,228
E 213
PZ 24,88,224
E 215
PV 24,120,228
E 216
PZ 28,92,224
MU 88,250
C 232,208
PV 28,124,228
MU 73,250
C 233,210
SZ 232,224
E 218
SM 232,228
MU 67,250
C 234,212
.LSTEP9:
QZ 208,0,208
E 217
QK 208,4,212
E 219
QK 208,8,216
v_permlane16_swap_b32 v40, v42
C 235,214
QK 208,12,220
E 220
QZ 212,16,208
E 222
QK 212,20,212
v_rcp_f32 v251, v251
QK 212,24,216
v_permlane16_swap_b32 v48, v50
C 236,216
QK 212,28,220
C 237,218
MU 68,250
QZ 216,32,208
E 221
QK 216,36,212
MU 96,251
C 238,220
QK 216,40,216
E 223
v_permlane16_swap_b32 v56, v58
QK 216,44,220
C 239,222
MU 81,250
QZ 220,48,208
MU 74,250
MU 69,250
QK 220,52,212
v_permlane16_swap_b32 v41, v43
MU 104,251
QK 220,56,216
MU 97,251
v_permlane16_swap_b32 v49, v51
QK 220,60,220
MU 112,251
v_permlane16_swap_b32 v57, v59
PZ 32,64,232
buffer_store_dwordx4 v[40:43], v247, s[76:79], s97 offen offset:64 sc0 sc1
PV 32,96,236
VR 64,242,16384
VR 66,242,20480
PZ 36,68,232
VR 96,242,24576
MU 70,250
PV 36,100,236
VR 98,242,28672
MU 98,251
PZ 40,72,232
VR 68,243,16384
MU 89,250
PV 40,104,236
VR 70,243,20480
MU 75,250
PZ 44,76,232
buffer_store_dwordx4 v[48:51], v247, s[76:79], s97 offen offset:128 sc0 sc1
PV 44,108,236
VR 100,243,24576
VR 102,243,28672
PZ 48,80,232
VR 72,242,16640
MU 120,251
PV 48,112,236
VR 74,242,20736
MU 105,251
PZ 52,84,232
buffer_store_dwordx4 v[56:59], v247, s[76:79], s97 offen offset:192 sc0 sc1
PV 52,116,236
VR 104,242,24832
VR 106,242,28928
PZ 56,88,232
VR 76,243,16640
MU 71,250
PV 56,120,236
VR 78,243,20736
MU 99,251
PZ 60,92,232
VR 108,243,24832
MU 82,250
PV 60,124,236
E 192
E 194
SZ 236,232
E 193
E 195
SM 236,236
C 224,192
C 225,194
QZ 192,0,128
VR 110,243,28928
VR 80,242,16896
QK 192,4,132
E 196
E 198
QK 192,8,136
E 197
E 199
QK 192,12,140
C 226,196
C 227,198
QZ 196,16,128
VR 82,242,20992
VR 112,242,25088
QK 196,20,132
E 200
E 202
QK 196,24,136
E 201
E 203
C 64,64
QK 196,28,140
C 228,200
C 229,202
MU 100,251
QZ 200,32,128
VR 114,242,29184
VR 84,243,16896
QK 200,36,132
E 204
E 206
MU 76,250
QK 200,40,136
E 205
E 207
QK 200,44,140
C 230,204
C 231,206
MU 113,251
QZ 204,48,128
MU 106,251
s_waitcnt lgkmcnt(2)
QK 204,52,132
VR 86,243,20992
VR 116,243,25088
QK 204,56,136
C 65,66
VR 118,243,29184
QK 204,60,140
MU 101,251
VR 88,242,17152
PV 128,64,224
VR 90,242,21248
MU 90,250
PV 128,96,228
VR 120,242,25344
C 66,68
PV 132,68,224
VR 122,242,29440
MU 83,250
PV 132,100,228
VR 92,243,17152
MU 77,250
PV 136,72,224
VR 94,243,21248
s_waitcnt lgkmcnt(9)
PV 136,104,228
VR 124,243,25344
MU 102,251
PV 140,76,224
VR 126,243,29440
s_waitcnt lgkmcnt(8)
PV 140,108,228
E 208
PV 144,80,224
E 210
PV 144,112,228
E 209
PV 148,84,224
MU 121,251
s_waitcnt lgkmcnt(4)
PV 148,116,228
E 211
PV 152,88,224
MU 107,251
s_waitcnt lgkmcnt(0)
PV 152,120,228
E 212
PV 156,92,224
C 67,70
C 232,208
PV 156,124,228
E 214
SM 224,224
MU 103,251
C 233,210
SM 224,228
s_waitcnt vmcnt(7)
s_barrier
s_add_u32 s84, s80, 49152
s_add_u32 m0, s80, 147456
QZ 208,0,144
buffer_load_dwordx4 v244, s[72:75], s84 offen offset:0  lds
MU 114,251
QK 208,4,148
KR 0,240,32768
s_mov_b32 s60, .LSTEP11-.LCODE0
QK 208,8,152
E 213
E 215
QK 208,12,156
C 234,212
C 235,214
QZ 212,16,144
v_permlane16_swap_b32 v64, v66
KR 4,240,33792
QK 212,20,148
E 216
E 218
QK 212,24,152
E 217
E 219
MU 78,250
C 96,96
QK 212,28,156
C 236,216
C 237,218
buffer_load_dwordx4 v245, s[72:75], s84 offen offset:1024  lds
QZ 216,32,144
KR 8,240,34816
MU 108,251
QK 216,36,148
E 220
E 222
QK 216,40,152
E 221
E 223
QK 216,44,156
C 238,220
C 239,222
MU 84,250
QZ 220,48,144
KR 12,240,35840
v_permlane16_swap_b32 v65, v67
QK 220,52,148
buffer_load_dwordx4 v244, s[72:75], s84 offen offset:2048  lds
QK 220,56,152
C 97,98
KR 16,240,36864
QK 220,60,156
buffer_store_dwordx4 v[64:67], v247, s[76:79], s98 offen offset:0 sc0 sc1
PV 160,64,232
KR 20,240,37888
MU 91,250
PV 160,96,236
MU 79,250
MU 122,251
PV 164,68,232
KR 24,240,38912
C 98,100
PV 164,100,236
E 192
PV 168,72,232
KR 28,240,39936
MU 115,251
PV 168,104,236
buffer_load_dwordx4 v245, s[72:75], s84 offen offset:3072  lds
PV 172,76,232
KR 32,240,40960
MU 109,251
PV 172,108,236
E 194
PV 176,80,232
KR 36,240,41984
C 99,102
PV 176,112,236
E 193
PV 180,84,232
KR 40,240,43008
C 224,192
PV 180,116,236
buffer_load_dword a244, v252, s[56:59], s60 offen
PV 184,88,232
KR 44,240,44032
MU 85,250
PV 184,120,236
E 195
PV 188,92,232
KR 48,240,45056
C 225,194
PV 188,124,236
E 196
SM 228,232
KR 52,240,46080
s_waitcnt lgkmcnt(5)
SM 228,236
E 198
QZ 192,0,192
KR 56,240,47104
C 72,72
QK 192,4,196
E 197
QK 192,8,200
E 199
QK 192,12,204
C 226,196
C 227,198
QZ 196,16,192
v_permlane16_swap_b32 v96, v98
s_waitcnt lgkmcnt(3)
QK 196,20,196
E 200
E 202
QK 196,24,200
E 201
E 203
MU 110,251
QK 196,28,204
C 228,200
C 229,202
KR 60,240,48128
QZ 200,32,192
E 204
QK 200,36,196
E 206
QK 200,40,200
E 205
E 207
QK 200,44,204
C 230,204
C 231,206
s_waitcnt lgkmcnt(0)
MU 116,251
QZ 204,48,192
MU 92,250
v_permlane16_swap_b32 v97, v99
QK 204,52,196
buffer_store_dwordx4 v[96:99], v247, s[76:79], s99 offen offset:0 sc0 sc1
QK 204,56,200
C 73,74
MU 123,251
QK 204,60,204
MU 111,251
MU 86,250
PV 0,64,224
E 208
PV 0,96,228
E 210
PV 4,68,224
E 209
PV 4,100,228
E 211
PV 8,72,224
C 232,208
C 233,210
PV 8,104,228
C 74,76
MU 117,251
PV 12,76,224
E 212
PV 12,108,228
E 214
PV 16,80,224
E 213
PV 16,112,228
E 215
PV 20,84,224
C 234,212
C 235,214
PV 20,116,228
E 216
PV 24,88,224
E 218
PV 24,120,228
E 217
PV 28,92,224
E 219
PV 28,124,228
E 220
SM 232,224
E 222
SM 232,228
E 221
.LSTEP10:
QZ 208,0,208
E 223
QK 208,4,212
C 236,216
C 237,218
QK 208,8,216
C 104,104
C 238,220
QK 208,12,220
C 239,222
MU 93,250
QZ 212,16,208
MU 87,250
MU 124,251
QK 212,20,212
C 75,78
C 105,106
QK 212,24,216
MU 118,251
v_permlane16_swap_b32 v72, v74
QK 212,28,220
E 192
QZ 216,32,208
E 194
QK 216,36,212
E 193
QK 216,40,216
E 195
QK 216,44,220
C 224,192
C 225,194
QZ 220,48,208
C 80,80
C 106,108
QK 220,52,212
E 196
QK 220,56,216
E 198
QK 220,60,220
E 197
PV 32,64,232
E 199
PV 32,96,236
VR 64,242,0
C 226,196
PV 36,68,232
VR 66,242,4096
C 227,198
PV 36,100,236
VR 96,242,8192
VR 98,242,12288
PV 40,72,232
VR 68,243,0
MU 94,250
PV 40,104,236
VR 70,243,4096
MU 125,251
PV 44,76,232
VR 100,243,8192
MU 119,251
PV 44,108,236
VR 102,243,12288
v_permlane16_swap_b32 v73, v75
PV 48,80,232
VR 72,242,256
C 107,110
PV 48,112,236
VR 74,242,4352
C 81,82
PV 52,84,232
VR 104,242,8448
v_permlane16_swap_b32 v104, v106
PV 52,116,236
VR 106,242,12544
C 112,112
PV 56,88,232
VR 76,243,256
MU 95,250
PV 56,120,236
VR 78,243,4352
VR 108,243,8448
PV 60,92,232
VR 110,243,12544
MU 126,251
PV 60,124,236
VR 80,242,512
C 82,84
SM 236,232
VR 82,242,4608
v_permlane16_swap_b32 v105, v107
SM 236,236
VR 112,242,8704
C 113,114
QZ 192,0,128
VR 114,242,12800
C 88,88
QK 192,4,132
VR 84,243,512
C 83,86
QK 192,8,136
VR 86,243,4608
MU 127,251
QK 192,12,140
VR 116,243,8704
C 114,116
QZ 196,16,128
VR 118,243,12800
v_permlane16_swap_b32 v80, v82
QK 196,20,132
E 200
E 202
QK 196,24,136
E 201
E 203
QK 196,28,140
C 228,200
C 229,202
QZ 200,32,128
VR 88,242,768
VR 90,242,4864
QK 200,36,132
E 204
E 206
QK 200,40,136
E 205
E 207
QK 200,44,140
C 230,204
C 231,206
QZ 204,48,128
buffer_store_dwordx4 v[72:75], v247, s[76:79], s98 offen offset:64 sc0 sc1
QK 204,52,132
VR 120,242,8960
s_waitcnt lgkmcnt(3)
QK 204,56,136
VR 122,242,13056
VR 92,243,768
QK 204,60,140
VR 94,243,4864
C 89,90
PV 128,64,224
VR 124,243,8960
C 120,120
PV 128,96,228
VR 126,243,13056
C 115,118
PV 132,68,224
E 208
PV 132,100,228
E 210
PV 136,72,224
E 209
PV 136,104,228
E 211
PV 140,76,224
C 232,208
C 233,210
PV 140,108,228
E 212
PV 144,80,224
E 214
PV 144,112,228
E 213
PV 148,84,224
E 215
PV 148,116,228
s_waitcnt lgkmcnt(1)
s_waitcnt lgkmcnt(0)
PV 152,88,224
E 216
PV 152,120,228
E 218
PV 156,92,224
E 217
PV 156,124,228
E 219
SM 224,224
E 220
SM 224,228
s_waitcnt vmcnt(3)
s_barrier
s_add_u32 s84, s80, 65536
s_add_u32 m0, s80, 131072
QZ 208,0,144
buffer_load_dwordx4 v244, s[72:75], s84 offen offset:0  lds
QK 208,4,148
KR 0,240,49152
C 234,212
QK 208,8,152
E 222
QK 208,12,156
C 235,214
C 236,216
QZ 212,16,144
KR 4,240,50176
C 237,218
QK 212,20,148
E 221
QK 212,24,152
buffer_load_dwordx4 v245, s[72:75], s84 offen offset:1024  lds
QK 212,28,156
KR 8,240,51200
C 238,220
QZ 216,32,144
E 223
QK 216,36,148
KR 12,240,52224
C 239,222
QK 216,40,152
s_mov_b32 s60, .LSTEP12-.LCODE0
v_permlane16_swap_b32 v81, v83
QK 216,44,156
KR 16,240,53248
C 90,92
QZ 220,48,144
buffer_store_dwordx4 v[104:107], v247, s[76:79], s99 offen offset:64 sc0 sc1
QK 220,52,148
KR 20,240,54272
v_permlane16_swap_b32 v112, v114
QK 220,56,152
buffer_load_dwordx4 v244, s[72:75], s84 offen offset:2048  lds
QK 220,60,156
KR 24,240,55296
C 121,122
PV 160,64,232
E 192
PV 160,96,236
KR 28,240,56320
v_permlane16_swap_b32 v113, v115
PV 164,68,232
E 194
PV 164,100,236
KR 32,240,57344
C 91,94
PV 168,72,232
buffer_load_dwordx4 v245, s[72:75], s84 offen offset:3072  lds
PV 168,104,236
KR 36,240,58368
C 122,124
PV 172,76,232
E 193
PV 172,108,236
KR 40,240,59392
C 224,192
PV 176,80,232
E 195
PV 176,112,236
KR 44,240,60416
C 225,194
PV 180,84,232
buffer_load_dword a244, v252, s[56:59], s60 offen
PV 180,116,236
KR 48,240,61440
v_permlane16_swap_b32 v88, v90
PV 184,88,232
E 196
PV 184,120,236
KR 52,240,62464
C 123,126
PV 188,92,232
E 198
PV 188,124,236
KR 56,240,63488
s_waitcnt lgkmcnt(5)
SM 228,232
E 197
SM 228,236
KR 60,240,64512
C 226,196
QZ 192,0,192
E 199
QK 192,4,196
E 200
QK 192,8,200
E 202
QK 192,12,204
C 227,198
v_permlane16_swap_b32 v89, v91
QZ 196,16,192
E 201
QK 196,20,196
E 203
QK 196,24,200
E 204
QK 196,28,204
C 228,200
C 229,202
s_waitcnt lgkmcnt(1)
QZ 200,32,192
E 206
QK 200,36,196
E 205
QK 200,40,200
E 207
QK 200,44,204
C 230,204
C 231,206
s_waitcnt lgkmcnt(0)
QZ 204,48,192
E 208
QK 204,52,196
E 210
QK 204,56,200
E 209
QK 204,60,204
E 211
PV 0,64,224
C 232,208
C 233,210
PV 0,96,228
E 212
PV 4,68,224
E 214
PV 4,100,228
E 213
PV 8,72,224
E 215
PV 8,104,228
C 234,212
C 235,214
PV 12,76,224
E 216
PV 12,108,228
E 218
PV 16,80,224
E 217
PV 16,112,228
E 219
PV 20,84,224
C 236,216
C 237,218
PV 20,116,228
E 220
PV 24,88,224
E 222
PV 24,120,228
E 221
PV 28,92,224
E 223
PV 28,124,228
C 238,220
C 239,222
SM 232,224
E 192
SM 232,228
E 194
.LSTEP11:
QZ 208,0,208
E 193
QK 208,4,212
E 195
QK 208,8,216
C 224,192
C 225,194
QK 208,12,220
E 196
QZ 212,16,208
E 198
QK 212,20,212
E 197
QK 212,24,216
E 199
QK 212,28,220
C 226,196
C 227,198
QZ 216,32,208
E 200
QK 216,36,212
buffer_store_dwordx4 v[80:83], v247, s[76:79], s98 offen offset:128 sc0 sc1
QK 216,40,216
E 202
QK 216,44,220
E 201
QZ 220,48,208
E 203
QK 220,52,212
C 228,200
C 229,202
QK 220,56,216
E 204
QK 220,60,220
E 206
PV 32,64,232
E 205
PV 32,96,236
VR 64,242,16384
VR 66,242,20480
PV 36,68,232
VR 96,242,24576
C 230,204
PV 36,100,236
VR 98,242,28672
v_permlane16_swap_b32 v120, v122
PV 40,72,232
VR 68,243,16384
v_permlane16_swap_b32 v121, v123
PV 40,104,236
VR 70,243,20480
PV 44,76,232
VR 100,243,24576
PV 44,108,236
VR 102,243,28672
PV 48,80,232
VR 72,242,16640
PV 48,112,236
VR 74,242,20736
PV 52,84,232
VR 104,242,24832
PV 52,116,236
VR 106,242,28928
VR 76,243,16640
PV 56,88,232
VR 78,243,20736
PV 56,120,236
VR 108,243,24832
PV 60,92,232
VR 110,243,28928
PV 60,124,236
VR 80,242,16896
SM 236,232
VR 82,242,20992
SM 236,236
VR 112,242,25088
QZ 192,0,128
VR 114,242,29184
QK 192,4,132
VR 84,243,16896
QK 192,8,136
VR 86,243,20992
QK 192,12,140
VR 116,243,25088
VR 118,243,29184
QZ 196,16,128
VR 88,242,17152
QK 196,20,132
VR 90,242,21248
QK 196,24,136
VR 120,242,25344
QK 196,28,140
VR 122,242,29440
QZ 200,32,128
VR 92,243,17152
QK 200,36,132
buffer_store_dwordx4 v[112:115], v247, s[76:79], s99 offen offset:128 sc0 sc1
QK 200,40,136
E 207
QK 200,44,140
C 231,206
VR 94,243,21248
QZ 204,48,128
VR 124,243,25344
s_waitcnt lgkmcnt(10)
QK 204,52,132
VR 126,243,29440
QK 204,56,136
E 208
QK 204,60,140
E 210
PV 128,64,224
E 209
PV 128,96,228
E 211
PV 132,68,224
C 232,208
C 233,210
PV 132,100,228
E 212
PV 136,72,224
E 214
PV 136,104,228
E 213
PV 140,76,224
s_waitcnt lgkmcnt(2)
C 234,212
PV 140,108,228
E 215
PV 144,80,224
E 216
PV 144,112,228
E 218
PV 148,84,224
s_waitcnt lgkmcnt(0)
C 235,214
PV 148,116,228
E 217
PV 152,88,224
E 219
PV 152,120,228
s_add_u32 s84, s80, 81920
s_add_u32 m0, s80, 147456
PV 156,92,224
C 236,216
C 237,218
PV 156,124,228
E 220
SM 224,224
E 222
SM 224,228
s_waitcnt vmcnt(3)
s_barrier
QZ 208,0,144
buffer_load_dwordx4 v244, s[72:75], s84 offen offset:0  lds
QK 208,4,148
KR 0,241,0
s_mov_b32 s60, .LSTEP13-.LCODE0
QK 208,8,152
E 221
QK 208,12,156
KR 4,241,1024
C 238,220
QZ 212,16,144
E 223
QK 212,20,148
KR 8,241,2048
C 239,222
QK 212,24,152
buffer_load_dwordx4 v245, s[72:75], s84 offen offset:1024  lds
QK 212,28,156
KR 12,241,3072
QZ 216,32,144
E 192
QK 216,36,148
KR 16,241,4096
QK 216,40,152
E 194
QK 216,44,156
KR 20,241,5120
QZ 220,48,144
buffer_load_dwordx4 v244, s[72:75], s84 offen offset:2048  lds
QK 220,52,148
KR 24,241,6144
QK 220,56,152
E 193
QK 220,60,156
KR 28,241,7168
C 224,192
PV 160,64,232
E 195
PV 160,96,236
KR 32,241,8192
C 225,194
PV 164,68,232
E 196
PV 164,100,236
buffer_load_dwordx4 v245, s[72:75], s84 offen offset:3072  lds
PV 168,72,232
KR 36,241,9216
PV 168,104,236
E 198
PV 172,76,232
KR 40,241,10240
PV 172,108,236
E 197
PV 176,80,232
KR 44,241,11264
s_add_u32 s86, s81, 131072
PV 176,112,236
E 199
PV 180,84,232
buffer_load_dword a244, v252, s[56:59], s60 offen
PV 180,116,236
KR 48,241,12288
C 226,196
PV 184,88,232
E 200
PV 184,120,236
KR 52,241,13312
C 227,198
PV 188,92,232
E 202
PV 188,124,236
KR 56,241,14336
s_waitcnt lgkmcnt(5)
SM 228,232
E 201
SM 228,236
buffer_load_dwordx4 a[160:163], v246, s[64:67], s86 offen offset:0
QZ 192,0,192
KR 60,241,15360
C 228,200
QK 192,4,196
E 203
QK 192,8,200
E 204
QK 192,12,204
E 206
QZ 196,16,192
E 205
QK 196,20,196
E 207
QK 196,24,200
buffer_load_dwordx4 a[164:167], v246, s[64:67], s86 offen offset:64
QK 196,28,204
C 229,202
s_waitcnt lgkmcnt(2)
QZ 200,32,192
C 230,204
C 231,206
QK 200,36,196
buffer_store_dwordx4 v[88:91], v247, s[76:79], s98 offen offset:192 sc0 sc1
QK 200,40,200
E 208
QK 200,44,204
s_waitcnt lgkmcnt(0)
QZ 204,48,192
E 210
QK 204,52,196
E 209
QK 204,56,200
E 211
QK 204,60,204
buffer_load_dwordx4 a[168:171], v246, s[64:67], s86 offen offset:128
PV 0,64,224
C 232,208
C 233,210
PV 0,96,228
E 212
PV 4,68,224
E 214
PV 4,100,228
E 213
PV 8,72,224
E 215
PV 8,104,228
buffer_load_dwordx4 a[172:175], v246, s[64:67], s86 offen offset:192
PV 12,76,224
C 234,212
C 235,214
PV 12,108,228
E 216
PV 16,80,224
E 218
PV 16,112,228
E 217
PV 20,84,224
E 219
PV 20,116,228
C 236,216
C 237,218
PV 24,88,224
E 220
PV 24,120,228
E 222
PV 28,92,224
E 221
PV 28,124,228
E 223
SM 232,224
C 238,220
C 239,222
SM 232,228
E 192
.LSTEP12:
QZ 208,0,208
E 194
QK 208,4,212
E 193
QK 208,8,216
E 195
QK 208,12,220
C 224,192
C 225,194
QZ 212,16,208
E 196
QK 212,20,212
E 198
QK 212,24,216
E 197
QK 212,28,220
buffer_store_dwordx4 v[120:123], v247, s[76:79], s99 offen offset:192 sc0 sc1
QZ 216,32,208
E 199
QK 216,36,212
C 226,196
C 227,198
QK 216,40,216
E 200
QK 216,44,220
E 202
QZ 220,48,208
E 201
QK 220,52,212
E 203
QK 220,56,216
C 228,200
C 229,202
QK 220,60,220
E 204
PV 32,64,232
E 206
PV 32,96,236
VR 64,242,0
VR 66,242,4096
PV 36,68,232
VR 96,242,8192
PV 36,100,236
VR 98,242,12288
PV 40,72,232
VR 68,243,0
PV 40,104,236
VR 70,243,4096
PV 44,76,232
VR 100,243,8192
PV 44,108,236
VR 102,243,12288
PV 48,80,232
VR 72,242,256
PV 48,112,236
VR 74,242,4352
PV 52,84,232
VR 104,242,8448
PV 52,116,236
VR 106,242,12544
VR 76,243,256
PV 56,88,232
VR 78,243,4352
PV 56,120,236
VR 108,243,8448
PV 60,92,232
VR 110,243,12544
PV 60,124,236
VR 80,242,512
SM 236,232
VR 82,242,4608
SM 236,236
VR 112,242,8704
QZ 192,0,128
VR 114,242,12800
QK 192,4,132
VR 84,243,512
QK 192,8,136
VR 86,243,4608
QK 192,12,140
VR 116,243,8704
VR 118,243,12800
QZ 196,16,128
VR 88,242,768
QK 196,20,132
VR 90,242,4864
QK 196,24,136
VR 120,242,8960
QK 196,28,140
VR 122,242,13056
QZ 200,32,128
VR 92,243,768
QK 200,36,132
VR 94,243,4864
QK 200,40,136
E 205
E 207
QK 200,44,140
C 230,204
C 231,206
QZ 204,48,128
VR 124,243,8960
s_waitcnt lgkmcnt(9)
QK 204,52,132
VR 126,243,13056
QK 204,56,136
E 208
QK 204,60,140
E 210
PV 128,64,224
E 209
PV 128,96,228
E 211
PV 132,68,224
C 232,208
C 233,210
PV 132,100,228
E 212
PV 136,72,224
E 214
PV 136,104,228
E 213
PV 140,76,224
s_waitcnt lgkmcnt(2)
C 234,212
PV 140,108,228
E 215
PV 144,80,224
E 216
PV 144,112,228
E 218
PV 148,84,224
s_waitcnt lgkmcnt(0)
C 235,214
PV 148,116,228
E 217
PV 152,88,224
E 219
PV 152,120,228
s_add_u32 s84, s80, 98304
s_add_u32 m0, s80, 131072
PV 156,92,224
C 236,216
C 237,218
PV 156,124,228
E 220
SM 224,224
E 222
SM 224,228
s_waitcnt vmcnt(7)
s_barrier
QZ 208,0,144
buffer_load_dwordx4 v244, s[72:75], s84 offen offset:0  lds
QK 208,4,148
KR 0,241,16384
s_mov_b32 s60, .LSTEP14-.LCODE0
QK 208,8,152
E 221
QK 208,12,156
KR 4,241,17408
C 238,220
QZ 212,16,144
E 223
QK 212,20,148
KR 8,241,18432
C 239,222
QK 212,24,152
buffer_load_dwordx4 v245, s[72:75], s84 offen offset:1024  lds
QK 212,28,156
KR 12,241,19456
QZ 216,32,144
E 192
QK 216,36,148
KR 16,241,20480
QK 216,40,152
E 194
QK 216,44,156
KR 20,241,21504
QZ 220,48,144
buffer_load_dwordx4 v244, s[72:75], s84 offen offset:2048  lds
QK 220,52,148
KR 24,241,22528
QK 220,56,152
E 193
QK 220,60,156
KR 28,241,23552
C 224,192
PV 160,64,232
E 195
PV 160,96,236
KR 32,241,24576
C 225,194
PV 164,68,232
E 196
PV 164,100,236
buffer_load_dwordx4 v245, s[72:75], s84 offen offset:3072  lds
PV 168,72,232
KR 36,241,25600
PV 168,104,236
E 198
PV 172,76,232
KR 40,241,26624
PV 172,108,236
E 197
PV 176,80,232
KR 44,241,27648
s_add_u32 s86, s81, 135168
PV 176,112,236
E 199
PV 180,84,232
buffer_load_dword a244, v252, s[56:59], s60 offen
PV 180,116,236
KR 48,241,28672
C 226,196
PV 184,88,232
E 200
PV 184,120,236
KR 52,241,29696
C 227,198
PV 188,92,232
E 202
PV 188,124,236
KR 56,241,30720
s_waitcnt lgkmcnt(5)
SM 228,232
E 201
SM 228,236
buffer_load_dwordx4 a[176:179], v246, s[64:67], s86 offen offset:0
QZ 192,0,192
KR 60,241,31744
C 228,200
QK 192,4,196
E 203
QK 192,8,200
E 204
QK 192,12,204
E 206
QZ 196,16,192
E 205
QK 196,20,196
E 207
QK 196,24,200
buffer_load_dwordx4 a[180:183], v246, s[64:67], s86 offen offset:64
QK 196,28,204
C 229,202
s_waitcnt lgkmcnt(2)
QZ 200,32,192
C 230,204
C 231,206
QK 200,36,196
E 208
QK 200,40,200
E 210
QK 200,44,204
s_waitcnt lgkmcnt(0)
QZ 204,48,192
E 209
QK 204,52,196
buffer_load_dwordx4 a[184:187], v246, s[64:67], s86 offen offset:128
QK 204,56,200
E 211
QK 204,60,204
C 232,208
C 233,210
PV 0,64,224
E 212
PV 0,96,228
E 214
PV 4,68,224
E 213
PV 4,100,228
E 215
PV 8,72,224
buffer_load_dwordx4 a[188:191], v246, s[64:67], s86 offen offset:192
PV 8,104,228
C 234,212
C 235,214
PV 12,76,224
E 216
PV 12,108,228
E 218
PV 16,80,224
E 217
PV 16,112,228
E 219
PV 20,84,224
C 236,216
C 237,218
PV 20,116,228
E 220
PV 24,88,224
E 222
PV 24,120,228
E 221
PV 28,92,224
E 223
PV 28,124,228
C 238,220
C 239,222
SM 232,224
E 192
SM 232,228
E 194
.LSTEP13:
QZ 208,0,208
E 193
QK 208,4,212
E 195
QK 208,8,216
C 224,192
C 225,194
QK 208,12,220
E 196
QZ 212,16,208
E 198
QK 212,20,212
E 197
QK 212,24,216
E 199
QK 212,28,220
C 226,196
C 227,198
QZ 216,32,208
E 200
QK 216,36,212
E 202
QK 216,40,216
E 201
QK 216,44,220
E 203
QZ 220,48,208
C 228,200
C 229,202
QK 220,52,212
E 204
QK 220,56,216
E 206
QK 220,60,220
E 205
PV 32,64,232
E 207
PV 32,96,236
VR 64,242,16384
VR 66,242,20480
PV 36,68,232
VR 96,242,24576
C 230,204
PV 36,100,236
VR 98,242,28672
C 231,206
PV 40,72,232
VR 68,243,16384
PV 40,104,236
VR 70,243,20480
PV 44,76,232
VR 100,243,24576
PV 44,108,236
VR 102,243,28672
PV 48,80,232
VR 72,242,16640
PV 48,112,236
VR 74,242,20736
PV 52,84,232
VR 104,242,24832
PV 52,116,236
VR 106,242,28928
VR 76,243,16640
PV 56,88,232
VR 78,243,20736
PV 56,120,236
VR 108,243,24832
PV 60,92,232
VR 110,243,28928
PV 60,124,236
VR 80,242,16896
SM 236,232
VR 82,242,20992
SM 236,236
VR 112,242,25088
QZ 192,0,128
VR 114,242,29184
QK 192,4,132
VR 84,243,16896
QK 192,8,136
VR 86,243,20992
QK 192,12,140
VR 116,243,25088
VR 118,243,29184
QZ 196,16,128
VR 88,242,17152
QK 196,20,132
VR 90,242,21248
QK 196,24,136
VR 120,242,25344
QK 196,28,140
VR 122,242,29440
QZ 200,32,128
VR 92,243,17152
QK 200,36,132
VR 94,243,21248
QK 200,40,136
VR 124,243,25344
QK 200,44,140
VR 126,243,29440
QZ 204,48,128
E 208
QK 204,52,132
E 210
QK 204,56,136
s_waitcnt lgkmcnt(10)
QK 204,60,140
E 209
PV 128,64,224
E 211
PV 128,96,228
C 232,208
C 233,210
PV 132,68,224
E 212
PV 132,100,228
E 214
PV 136,72,224
E 213
PV 136,104,228
E 215
PV 140,76,224
s_waitcnt lgkmcnt(1)
C 234,212
PV 140,108,228
E 216
PV 144,80,224
E 218
PV 144,112,228
E 217
PV 148,84,224
s_waitcnt lgkmcnt(0)
C 235,214
PV 148,116,228
E 219
PV 152,88,224
C 236,216
C 237,218
PV 152,120,228
s_add_u32 s84, s80, 114688
s_add_u32 m0, s80, 147456
PV 156,92,224
E 220
PV 156,124,228
E 222
SM 224,224
E 221
SM 224,228
s_waitcnt vmcnt(5)
s_barrier
QZ 208,0,144
buffer_load_dwordx4 v244, s[72:75], s84 offen offset:0  lds
QK 208,4,148
KR 0,241,32768
C 238,220
QK 208,8,152
E 223
QK 208,12,156
KR 4,241,33792
C 239,222
QZ 212,16,144
E 192
QK 212,20,148
KR 8,241,34816
s_mov_b32 s60, .LSTEP15-.LCODE0
QK 212,24,152
buffer_load_dwordx4 v245, s[72:75], s84 offen offset:1024  lds
QK 212,28,156
KR 12,241,35840
QZ 216,32,144
E 194
QK 216,36,148
KR 16,241,36864
QK 216,40,152
E 193
QK 216,44,156
KR 20,241,37888
C 224,192
QZ 220,48,144
buffer_load_dwordx4 v244, s[72:75], s84 offen offset:2048  lds
QK 220,52,148
KR 24,241,38912
QK 220,56,152
E 195
QK 220,60,156
KR 28,241,39936
C 225,194
PV 160,64,232
E 196
PV 160,96,236
KR 32,241,40960
PV 164,68,232
E 198
PV 164,100,236
buffer_load_dwordx4 v245, s[72:75], s84 offen offset:3072  lds
PV 168,72,232
KR 36,241,41984
PV 168,104,236
E 197
PV 172,76,232
KR 40,241,43008
C 226,196
PV 172,108,236
E 199
PV 176,80,232
KR 44,241,44032
s_add_u32 s86, s81, 139264
PV 176,112,236
E 200
PV 180,84,232
buffer_load_dword a244, v252, s[56:59], s60 offen
PV 180,116,236
KR 48,241,45056
C 227,198
PV 184,88,232
E 202
PV 184,120,236
KR 52,241,46080
PV 188,92,232
E 201
PV 188,124,236
KR 56,241,47104
s_waitcnt lgkmcnt(5)
SM 228,232
E 203
SM 228,236
buffer_load_dword a246, v255, s[64:67], s86 offen
QZ 192,0,192
KR 60,241,48128
C 228,200
QK 192,4,196
E 204
QK 192,8,200
E 206
QK 192,12,204
E 205
QZ 196,16,192
E 207
QK 196,20,196
C 229,202
s_waitcnt lgkmcnt(3)
QK 196,24,200
C 230,204
C 231,206
QK 196,28,204
E 208
QZ 200,32,192
E 210
QK 200,36,196
E 209
QK 200,40,200
s_waitcnt lgkmcnt(0)
C 232,208
QK 200,44,204
E 211
QZ 204,48,192
E 212
QK 204,52,196
E 214
QK 204,56,200
E 213
QK 204,60,204
C 233,210
C 234,212
PV 0,64,224
E 215
PV 0,96,228
E 216
PV 4,68,224
E 218
PV 4,100,228
E 217
PV 8,72,224
C 235,214
C 236,216
PV 8,104,228
E 219
PV 12,76,224
E 220
PV 12,108,228
E 222
PV 16,80,224
E 221
PV 16,112,228
C 237,218
C 238,220
PV 20,84,224
E 223
PV 20,116,228
E 192
PV 24,88,224
E 194
PV 24,120,228
E 193
PV 28,92,224
E 195
PV 28,124,228
E 196
SM 232,224
C 239,222
C 224,192
SM 232,228
E 198
.LSTEP14:
QZ 208,0,208
E 197
QK 208,4,212
C 225,194
C 226,196
QK 208,8,216
E 199
QK 208,12,220
E 200
QZ 212,16,208
E 202
QK 212,20,212
E 201
QK 212,24,216
C 227,198
C 228,200
QK 212,28,220
E 203
QZ 216,32,208
E 204
QK 216,36,212
E 206
QK 216,40,216
E 205
QK 216,44,220
C 229,202
C 230,204
QZ 220,48,208
E 207
QK 220,52,212
E 208
QK 220,56,216
E 210
QK 220,60,220
E 209
PV 32,64,232
E 211
PV 32,96,236
VR 64,242,0
VR 66,242,4096
PV 36,68,232
VR 96,242,8192
C 231,206
PV 36,100,236
VR 98,242,12288
PV 40,72,232
VR 68,243,0
PV 40,104,236
VR 70,243,4096
PV 44,76,232
VR 100,243,8192
PV 44,108,236
VR 102,243,12288
PV 48,80,232
VR 72,242,256
PV 48,112,236
VR 74,242,4352
PV 52,84,232
VR 104,242,8448
PV 52,116,236
VR 106,242,12544
VR 76,243,256
PV 56,88,232
VR 78,243,4352
PV 56,120,236
VR 108,243,8448
PV 60,92,232
VR 110,243,12544
PV 60,124,236
VR 80,242,512
SM 236,232
VR 82,242,4608
C 232,208
SM 236,236
VR 112,242,8704
C 233,210
QZ 192,0,128
VR 114,242,12800
QK 192,4,132
VR 84,243,512
QK 192,8,136
VR 86,243,4608
QK 192,12,140
VR 116,243,8704
VR 118,243,12800
QZ 196,16,128
VR 88,242,768
QK 196,20,132
VR 90,242,4864
QK 196,24,136
VR 120,242,8960
QK 196,28,140
VR 122,242,13056
QZ 200,32,128
VR 92,243,768
QK 200,36,132
VR 94,243,4864
QK 200,40,136
VR 124,243,8960
QK 200,44,140
VR 126,243,13056
QZ 204,48,128
E 212
QK 204,52,132
E 214
QK 204,56,136
s_waitcnt lgkmcnt(10)
QK 204,60,140
E 213
PV 128,64,224
E 215
PV 128,96,228
C 234,212
C 235,214
PV 132,68,224
E 216
PV 132,100,228
E 218
PV 136,72,224
E 217
PV 136,104,228
E 219
PV 140,76,224
s_waitcnt lgkmcnt(1)
C 236,216
PV 140,108,228
E 220
PV 144,80,224
E 222
PV 144,112,228
E 221
PV 148,84,224
s_waitcnt lgkmcnt(0)
C 237,218
PV 148,116,228
E 223
PV 152,88,224
C 238,220
C 239,222
PV 152,120,228
s_add_u32 s84, s80, 0
s_add_u32 m0, s80, 131072
PV 156,92,224
E 192
PV 156,124,228
E 194
SM 224,224
E 193
SM 224,228
s_waitcnt vmcnt(2)
s_barrier
QZ 208,0,144
buffer_load_dwordx4 v244, s[72:75], s84 offen offset:0  lds
QK 208,4,148
KR 0,241,49152
s_mov_b32 s60, .LSTEP16-.LCODE0
QK 208,8,152
E 195
QK 208,12,156
KR 4,241,50176
C 224,192
QZ 212,16,144
E 196
QK 212,20,148
KR 8,241,51200
C 225,194
QK 212,24,152
buffer_load_dwordx4 v245, s[72:75], s84 offen offset:1024  lds
QK 212,28,156
KR 12,241,52224
QZ 216,32,144
E 198
QK 216,36,148
KR 16,241,53248
QK 216,40,152
E 197
QK 216,44,156
KR 20,241,54272
C 226,196
QZ 220,48,144
buffer_load_dwordx4 v244, s[72:75], s84 offen offset:2048  lds
QK 220,52,148
KR 24,241,55296
QK 220,56,152
E 199
QK 220,60,156
KR 28,241,56320
C 227,198
PV 160,64,232
E 200
PV 160,96,236
KR 32,241,57344
PV 164,68,232
E 202
PV 164,100,236
buffer_load_dwordx4 v245, s[72:75], s84 offen offset:3072  lds
PV 168,72,232
KR 36,241,58368
PV 168,104,236
E 201
PV 172,76,232
KR 40,241,59392
C 228,200
PV 172,108,236
E 203
PV 176,80,232
KR 44,241,60416
C 229,202
PV 176,112,236
E 204
PV 180,84,232
buffer_load_dword a244, v252, s[56:59], s60 offen
PV 180,116,236
KR 48,241,61440
PV 184,88,232
E 206
PV 184,120,236
KR 52,241,62464
PV 188,92,232
E 205
PV 188,124,236
KR 56,241,63488
s_waitcnt lgkmcnt(5)
SM 228,232
E 207
SM 228,236
KR 60,241,64512
C 230,204
QZ 192,0,192
E 208
QK 192,4,196
E 210
QK 192,8,200
E 209
QK 192,12,204
C 231,206
C 232,208
QZ 196,16,192
E 211
QK 196,20,196
s_waitcnt lgkmcnt(3)
C 233,210
QK 196,24,200
E 212
QK 196,28,204
E 214
QZ 200,32,192
E 213
QK 200,36,196
s_waitcnt lgkmcnt(0)
C 234,212
QK 200,40,200
E 215
QK 200,44,204
E 216
QZ 204,48,192
s_add_u32 s86, s81, 139264
C 235,214
QK 204,52,196
E 218
QK 204,56,200
E 217
QK 204,60,204
E 219
PV 0,64,224
buffer_load_dwordx4 a[192:195], v246, s[64:67], s86 offen offset:0
PV 0,96,228
C 236,216
C 237,218
PV 4,68,224
E 220
PV 4,100,228
E 222
PV 8,72,224
E 221
PV 8,104,228
E 223
PV 12,76,224
buffer_load_dwordx4 a[196:199], v246, s[64:67], s86 offen offset:64
PV 12,108,228
C 238,220
C 239,222
PV 16,80,224
E 192
PV 16,112,228
E 194
PV 20,84,224
E 193
PV 20,116,228
E 195
PV 24,88,224
E 196
PV 24,120,228
buffer_load_dwordx4 a[200:203], v246, s[64:67], s86 offen offset:128
PV 28,92,224
E 198
PV 28,124,228
E 197
SM 232,224
C 224,192
C 225,194
SM 232,228
E 199
.LSTEP15:
QZ 208,0,208
C 226,196
C 227,198
QK 208,4,212
E 200
QK 208,8,216
buffer_load_dwordx4 a[204:207], v246, s[64:67], s86 offen offset:192
QK 208,12,220
E 202
QZ 212,16,208
E 201
QK 212,20,212
E 203
QK 212,24,216
C 228,200
C 229,202
QK 212,28,220
E 204
QZ 216,32,208
E 206
QK 216,36,212
E 205
QK 216,40,216
E 207
QK 216,44,220
C 230,204
C 231,206
QZ 220,48,208
E 208
QK 220,52,212
E 210
QK 220,56,216
E 209
QK 220,60,220
E 211
PV 32,64,232
s_add_u32 s86, s81, 143360
PV 32,96,236
buffer_load_dwordx4 a[208:211], v246, s[64:67], s86 offen offset:0
PV 36,68,232
VR 64,242,16384
VR 66,242,20480
PV 36,100,236
VR 96,242,24576
PV 40,72,232
VR 98,242,28672
PV 40,104,236
VR 68,243,16384
PV 44,76,232
VR 70,243,20480
PV 44,108,236
buffer_load_dwordx4 a[212:215], v246, s[64:67], s86 offen offset:64
PV 48,80,232
VR 100,243,24576
VR 102,243,28672
PV 48,112,236
VR 72,242,16640
PV 52,84,232
VR 74,242,20736
PV 52,116,236
VR 104,242,24832
PV 56,88,232
VR 106,242,28928
PV 56,120,236
buffer_load_dwordx4 a[216:219], v246, s[64:67], s86 offen offset:128
PV 60,92,232
VR 76,243,16640
VR 78,243,20736
PV 60,124,236
VR 108,243,24832
SM 236,232
VR 110,243,28928
C 232,208
SM 236,236
VR 80,242,16896
C 233,210
QZ 192,0,128
VR 82,242,20992
QK 192,4,132
VR 112,242,25088
QK 192,8,136
buffer_load_dwordx4 a[220:223], v246, s[64:67], s86 offen offset:192
QK 192,12,140
VR 114,242,29184
VR 84,243,16896
QZ 196,16,128
VR 86,243,20992
QK 196,20,132
VR 116,243,25088
QK 196,24,136
VR 118,243,29184
QK 196,28,140
VR 88,242,17152
QZ 200,32,128
VR 90,242,21248
QK 200,36,132
VR 120,242,25344
QK 200,40,136
VR 122,242,29440
QK 200,44,140
VR 92,243,17152
QZ 204,48,128
VR 94,243,21248
s_waitcnt lgkmcnt(11)
QK 204,52,132
VR 124,243,25344
VR 126,243,29440
QK 204,56,136
E 212
QK 204,60,140
E 214
PV 128,64,224
E 213
PV 128,96,228
E 215
PV 132,68,224
C 234,212
C 235,214
PV 132,100,228
E 216
PV 136,72,224
E 218
PV 136,104,228
E 217
PV 140,76,224
s_waitcnt lgkmcnt(3)
C 236,216
PV 140,108,228
E 219
PV 144,80,224
E 220
PV 144,112,228
E 222
PV 148,84,224
s_waitcnt lgkmcnt(0)
C 237,218
PV 148,116,228
E 221
PV 152,88,224
E 223
PV 152,120,228
s_add_u32 s84, s80, 16384
s_add_u32 m0, s80, 147456
PV 156,92,224
C 238,220
C 239,222
PV 156,124,228
E 192
SM 224,224
E 194
SM 224,228
s_waitcnt vmcnt(9)
s_barrier
QZ 208,0,144
buffer_load_dwordx4 v244, s[72:75], s84 offen offset:0  lds
QK 208,4,148
KR 0,240,0
s_mov_b32 s60, .LSTEP17-.LCODE0
QK 208,8,152
E 193
QK 208,12,156
KR 4,240,1024
v_accvgpr_read_b32 v248, a224
QZ 212,16,144
v_subrev_u32 v253, s88, v248
C 224,192
QK 212,20,148
KR 8,240,2048
v_max_u32 v254, v254, v253
QK 212,24,152
buffer_load_dwordx4 v245, s[72:75], s84 offen offset:1024  lds
QK 212,28,156
KR 12,240,3072
s_add_u32 s96, s81, 65536
QZ 216,32,144
v_rcp_f32 v248, v248
QK 216,36,148
KR 16,240,4096
MU 128,248
QK 216,40,152
MU 136,248
MU 144,248
QK 216,44,156
KR 20,240,5120
MU 152,248
QZ 220,48,144
buffer_load_dwordx4 v244, s[72:75], s84 offen offset:2048  lds
QK 220,52,148
KR 24,240,6144
MU 129,248
QK 220,56,152
MU 130,248
MU 137,248
QK 220,60,156
KR 28,240,7168
MU 145,248
PV 160,64,232
MU 153,248
MU 131,248
PV 160,96,236
KR 32,240,8192
MU 138,248
PV 164,68,232
E 195
PV 164,100,236
buffer_load_dwordx4 v245, s[72:75], s84 offen offset:3072  lds
PV 168,72,232
KR 36,240,9216
C 225,194
PV 168,104,236
E 196
PV 172,76,232
KR 40,240,10240
MU 132,248
PV 172,108,236
E 198
PV 176,80,232
KR 44,240,11264
MU 146,248
PV 176,112,236
E 197
PV 180,84,232
buffer_load_dword a244, v252, s[56:59], s60 offen
PV 180,116,236
KR 48,240,12288
C 226,196
PV 184,88,232
E 199
PV 184,120,236
KR 52,240,13312
C 227,198
PV 188,92,232
E 200
PV 188,124,236
KR 56,240,14336
s_waitcnt lgkmcnt(5)
SM 228,232
E 202
SM 228,236
KR 60,240,15360
MU 154,248
QZ 192,0,160
E 201
QK 192,4,164
E 203
QK 192,8,168
E 204
QK 192,12,172
E 206
QZ 196,16,160
E 205
QK 196,20,164
E 207
QK 196,24,168
C 228,200
C 229,202
QK 196,28,172
s_waitcnt lgkmcnt(2)
C 230,204
QZ 200,32,160
C 231,206
s_waitcnt lgkmcnt(0)
QK 200,36,164
v_accvgpr_read_b32 v249, a228
v_subrev_u32 v253, s88, v249
QK 200,40,168
v_rcp_f32 v249, v249
QK 200,44,172
MU 160,249
v_max_u32 v254, v254, v253
QZ 204,48,160
s_add_u32 s97, s81, 69632
MU 168,249
QK 204,52,164
MU 176,249
MU 184,249
QK 204,56,168
MU 133,248
MU 161,249
QK 204,60,172
MU 139,248
MU 147,248
PV 0,64,224
MU 155,248
MU 134,248
PV 0,96,228
MU 162,249
MU 169,249
PV 4,68,224
MU 177,249
MU 185,249
PV 4,100,228
E 208
PV 8,72,224
E 210
PV 8,104,228
E 209
PV 12,76,224
E 211
PV 12,108,228
E 212
PV 16,80,224
E 214
PV 16,112,228
E 213
PV 20,84,224
E 215
PV 20,116,228
E 216
PV 24,88,224
E 218
PV 24,120,228
E 217
PV 28,92,224
E 219
PV 28,124,228
E 220
SM 232,224
E 222
SM 232,228
C 232,208
C 233,210
.LSTEP16:
QZ 208,0,176
MU 135,248
MU 140,248
QK 208,4,180
MU 148,248
MU 163,249
QK 208,8,184
E 221
QK 208,12,188
C 234,212
C 235,214
MU 156,248
QZ 212,16,176
E 223
QK 212,20,180
C 236,216
C 237,218
QK 212,24,184
C 238,220
C 239,222
QK 212,28,188
v_accvgpr_read_b32 v250, a232
v_subrev_u32 v253, s88, v250
QZ 216,32,176
C 128,128
MU 170,249
QK 216,36,180
MU 164,249
MU 178,249
MU 186,249
QK 216,40,184
v_max_u32 v254, v254, v253
s_add_u32 s98, s81, 73728
QK 216,44,188
MU 141,248
MU 149,248
QZ 220,48,176
C 129,130
MU 157,248
QK 220,52,180
MU 165,249
MU 171,249
QK 220,56,184
MU 179,249
MU 187,249
QK 220,60,188
C 130,132
MU 142,248
PV 32,64,232
MU 166,249
MU 150,248
PV 32,96,236
VR 64,242,0
VR 66,242,4096
PV 36,68,232
VR 96,242,8192
MU 158,248
PV 36,100,236
VR 98,242,12288
C 131,134
PV 40,72,232
VR 68,243,0
MU 167,249
PV 40,104,236
VR 70,243,4096
MU 172,249
PV 44,76,232
VR 100,243,8192
MU 180,249
PV 44,108,236
VR 102,243,12288
MU 188,249
PV 48,80,232
VR 72,242,256
MU 143,248
PV 48,112,236
VR 74,242,4352
v_permlane16_swap_b32 v128, v130
PV 52,84,232
VR 104,242,8448
MU 151,248
PV 52,116,236
VR 106,242,12544
VR 76,243,256
PV 56,88,232
VR 78,243,4352
C 160,160
PV 56,120,236
VR 108,243,8448
MU 159,248
PV 60,92,232
VR 110,243,12544
MU 173,249
PV 60,124,236
E 192
E 194
SM 236,232
E 193
E 195
SM 236,236
C 224,192
C 225,194
s_waitcnt vmcnt(9)
QZ 192,0,192
VR 80,242,512
VR 82,242,4608
QK 192,4,196
E 196
E 198
QK 192,8,200
E 197
E 199
QK 192,12,204
C 226,196
C 227,198
QZ 196,16,192
VR 112,242,8704
VR 114,242,12800
QK 196,20,196
E 200
E 202
QK 196,24,200
E 201
E 203
QK 196,28,204
C 228,200
C 229,202
QZ 200,32,192
VR 84,243,512
VR 86,243,4608
QK 200,36,196
E 204
E 206
QK 200,40,200
E 205
E 207
QK 200,44,204
C 230,204
C 231,206
QZ 204,48,192
VR 116,243,8704
s_waitcnt lgkmcnt(3)
QK 204,52,196
VR 118,243,12800
VR 88,242,768
QK 204,56,200
VR 90,242,4864
v_accvgpr_read_b32 v251, a236
QK 204,60,204
v_permlane16_swap_b32 v129, v131
MU 181,249
PZ 64,64,224
C 161,162
MU 189,249
PV 64,96,228
buffer_store_dwordx4 v[128:131], v247, s[76:79], s96 offen offset:0 sc0 sc1
PZ 68,68,224
C 136,136
VR 120,242,8960
PV 68,100,228
VR 122,242,13056
VR 92,243,768
PZ 72,72,224
VR 94,243,4864
v_subrev_u32 v253, s88, v251
PV 72,104,228
C 144,144
VR 124,243,8960
PZ 76,76,224
VR 126,243,13056
s_waitcnt lgkmcnt(6)
PV 76,108,228
C 152,152
v_max_u32 v254, v254, v253
PZ 80,80,224
C 162,164
s_waitcnt lgkmcnt(4)
PV 80,112,228
E 208
PZ 84,84,224
MU 174,249
s_waitcnt lgkmcnt(0)
PV 84,116,228
E 210
PZ 88,88,224
MU 182,249
s_add_u32 s99, s81, 77824
PV 88,120,228
E 209
PZ 92,92,224
MU 190,249
C 232,208
PV 92,124,228
C 137,138
s_add_u32 s84, s80, 32768
SZ 224,224
E 211
C 163,166
SM 224,228
C 233,210
s_waitcnt vmcnt(6)
s_waitcnt vmcnt(2)
s_barrier
s_add_u32 m0, s80, 131072
QZ 208,0,208
buffer_load_dwordx4 v244, s[72:75], s84 offen offset:0  lds
C 145,146
QK 208,4,212
E 212
E 214
QK 208,8,216
E 213
E 215
QK 208,12,220
C 234,212
C 235,214
C 153,154
QZ 212,16,208
KR 0,240,16384
s_mov_b32 s60, .LSTEP18-.LCODE0
QK 212,20,212
E 216
E 218
MU 175,249
QK 212,24,216
E 217
E 219
v_permlane16_swap_b32 v160, v162
QK 212,28,220
C 236,216
C 237,218
buffer_load_dwordx4 v245, s[72:75], s84 offen offset:1024  lds
MU 183,249
QZ 216,32,208
KR 4,240,17408
C 138,140
QK 216,36,212
E 220
E 222
QK 216,40,216
E 221
E 223
MU 191,249
QK 216,44,220
C 238,220
C 239,222
QZ 220,48,208
KR 8,240,18432
C 146,148
QK 220,52,212
buffer_load_dwordx4 v244, s[72:75], s84 offen offset:2048  lds
QK 220,56,216
KR 12,240,19456
v_permlane16_swap_b32 v161, v163
QK 220,60,220
C 154,156
C 168,168
PZ 96,64,232
KR 16,240,20480
C 176,176
PV 96,96,236
buffer_store_dwordx4 v[160:163], v247, s[76:79], s97 offen offset:0 sc0 sc1
PZ 100,68,232
KR 20,240,21504
C 139,142
PV 100,100,236
E 192
PZ 104,72,232
KR 24,240,22528
C 184,184
PV 104,104,236
buffer_load_dwordx4 v245, s[72:75], s84 offen offset:3072  lds
PZ 108,76,232
KR 28,240,23552
C 147,150
PV 108,108,236
E 194
PZ 112,80,232
KR 32,240,24576
C 155,158
PV 112,112,236
E 193
PZ 116,84,232
KR 36,240,25600
C 224,192
PV 116,116,236
buffer_load_dword a244, v252, s[56:59], s60 offen
PZ 120,88,232
KR 40,240,26624
C 169,170
PV 120,120,236
E 195
PZ 124,92,232
KR 44,240,27648
C 225,194
PV 124,124,236
E 196
SZ 228,232
KR 48,240,28672
s_waitcnt lgkmcnt(5)
SM 228,236
E 198
QZ 192,0,160
C 177,178
KR 52,240,29696
QK 192,4,164
E 197
QK 192,8,168
E 199
v_permlane16_swap_b32 v136, v138
QK 192,12,172
C 226,196
C 227,198
C 185,186
QZ 196,16,160
KR 56,240,30720
s_waitcnt lgkmcnt(3)
QK 196,20,164
E 200
E 202
QK 196,24,168
E 201
E 203
v_permlane16_swap_b32 v144, v146
QK 196,28,172
C 228,200
C 229,202
KR 60,240,31744
QZ 200,32,160
E 204
QK 200,36,164
E 206
v_rcp_f32 v250, v250
s_nop 0
MU 0,250
QK 200,40,168
E 205
E 207
v_permlane16_swap_b32 v152, v154
C 170,172
QK 200,44,172
C 230,204
C 231,206
s_waitcnt lgkmcnt(0)
QZ 204,48,160
C 178,180
v_permlane16_swap_b32 v137, v139
QK 204,52,164
MU 8,250
C 186,188
QK 204,56,168
MU 1,250
v_permlane16_swap_b32 v145, v147
QK 204,60,172
C 171,174
MU 16,250
PZ 128,64,224
buffer_store_dwordx4 v[136:139], v247, s[76:79], s96 offen offset:64 sc0 sc1
PV 128,96,228
v_permlane16_swap_b32 v153, v155
MU 2,250
PZ 132,68,224
C 179,182
C 187,190
PV 132,100,228
E 208
PZ 136,72,224
E 210
PV 136,104,228
E 209
PZ 140,76,224
buffer_store_dwordx4 v[144:147], v247, s[76:79], s96 offen offset:128 sc0 sc1
PV 140,108,228
E 211
PZ 144,80,224
E 212
PV 144,112,228
E 214
PZ 148,84,224
buffer_store_dwordx4 v[152:155], v247, s[76:79], s96 offen offset:192 sc0 sc1
PV 148,116,228
E 213
PZ 152,88,224
E 215
PV 152,120,228
E 216
PZ 156,92,224
MU 24,250
C 232,208
PV 156,124,228
MU 9,250
C 233,210
SZ 232,224
E 218
SM 232,228
MU 3,250
C 234,212
.LSTEP17:
QZ 208,0,176
E 217
QK 208,4,180
E 219
QK 208,8,184
v_permlane16_swap_b32 v168, v170
C 235,214
QK 208,12,188
E 220
QZ 212,16,176
E 222
QK 212,20,180
v_rcp_f32 v251, v251
QK 212,24,184
v_permlane16_swap_b32 v176, v178
C 236,216
QK 212,28,188
C 237,218
MU 4,250
QZ 216,32,176
E 221
QK 216,36,180
MU 32,251
C 238,220
QK 216,40,184
E 223
v_permlane16_swap_b32 v184, v186
QK 216,44,188
C 239,222
MU 17,250
QZ 220,48,176
MU 10,250
MU 5,250
QK 220,52,180
v_permlane16_swap_b32 v169, v171
MU 40,251
QK 220,56,184
MU 33,251
v_permlane16_swap_b32 v177, v179
QK 220,60,188
MU 48,251
v_permlane16_swap_b32 v185, v187
PZ 160,64,232
buffer_store_dwordx4 v[168:171], v247, s[76:79], s97 offen offset:64 sc0 sc1
PV 160,96,236
VR 64,242,16384
VR 66,242,20480
PZ 164,68,232
VR 96,242,24576
MU 6,250
PV 164,100,236
VR 98,242,28672
MU 34,251
PZ 168,72,232
VR 68,243,16384
MU 25,250
PV 168,104,236
VR 70,243,20480
MU 11,250
PZ 172,76,232
buffer_store_dwordx4 v[176:179], v247, s[76:79], s97 offen offset:128 sc0 sc1
PV 172,108,236
VR 100,243,24576
VR 102,243,28672
PZ 176,80,232
VR 72,242,16640
MU 56,251
PV 176,112,236
VR 74,242,20736
MU 41,251
PZ 180,84,232
buffer_store_dwordx4 v[184:187], v247, s[76:79], s97 offen offset:192 sc0 sc1
PV 180,116,236
VR 104,242,24832
VR 106,242,28928
PZ 184,88,232
VR 76,243,16640
MU 7,250
PV 184,120,236
VR 78,243,20736
MU 35,251
PZ 188,92,232
VR 108,243,24832
MU 18,250
PV 188,124,236
E 192
E 194
SZ 236,232
E 193
E 195
SM 236,236
C 224,192
C 225,194
QZ 192,0,192
VR 110,243,28928
VR 80,242,16896
QK 192,4,196
E 196
E 198
QK 192,8,200
E 197
E 199
QK 192,12,204
C 226,196
C 227,198
QZ 196,16,192
VR 82,242,20992
VR 112,242,25088
QK 196,20,196
E 200
E 202
QK 196,24,200
E 201
E 203
C 0,0
QK 196,28,204
C 228,200
C 229,202
MU 36,251
QZ 200,32,192
VR 114,242,29184
VR 84,243,16896
QK 200,36,196
E 204
E 206
MU 12,250
QK 200,40,200
E 205
E 207
QK 200,44,204
C 230,204
C 231,206
MU 49,251
QZ 204,48,192
MU 42,251
s_waitcnt lgkmcnt(2)
QK 204,52,196
VR 86,243,20992
VR 116,243,25088
QK 204,56,200
C 1,2
VR 118,243,29184
QK 204,60,204
MU 37,251
VR 88,242,17152
PV 64,64,224
VR 90,242,21248
MU 26,250
PV 64,96,228
VR 120,242,25344
C 2,4
PV 68,68,224
VR 122,242,29440
MU 19,250
PV 68,100,228
VR 92,243,17152
MU 13,250
PV 72,72,224
VR 94,243,21248
s_waitcnt lgkmcnt(9)
PV 72,104,228
VR 124,243,25344
MU 38,251
PV 76,76,224
VR 126,243,29440
s_waitcnt lgkmcnt(8)
PV 76,108,228
E 208
PV 80,80,224
E 210
PV 80,112,228
E 209
PV 84,84,224
MU 57,251
s_waitcnt lgkmcnt(4)
PV 84,116,228
E 211
PV 88,88,224
MU 43,251
s_waitcnt lgkmcnt(0)
PV 88,120,228
E 212
PV 92,92,224
C 3,6
C 232,208
PV 92,124,228
E 214
SM 224,224
MU 39,251
C 233,210
SM 224,228
s_waitcnt vmcnt(7)
s_barrier
s_add_u32 s84, s80, 49152
s_add_u32 m0, s80, 147456
QZ 208,0,208
buffer_load_dwordx4 v244, s[72:75], s84 offen offset:0  lds
MU 50,251
QK 208,4,212
KR 0,240,32768
s_mov_b32 s60, .LSTEP19-.LCODE0
QK 208,8,216
E 213
E 215
QK 208,12,220
C 234,212
C 235,214
QZ 212,16,208
v_permlane16_swap_b32 v0, v2
KR 4,240,33792
QK 212,20,212
E 216
E 218
QK 212,24,216
E 217
E 219
MU 14,250
C 32,32
QK 212,28,220
C 236,216
C 237,218
buffer_load_dwordx4 v245, s[72:75], s84 offen offset:1024  lds
QZ 216,32,208
KR 8,240,34816
MU 44,251
QK 216,36,212
E 220
E 222
QK 216,40,216
E 221
E 223
QK 216,44,220
C 238,220
C 239,222
MU 20,250
QZ 220,48,208
KR 12,240,35840
v_permlane16_swap_b32 v1, v3
QK 220,52,212
buffer_load_dwordx4 v244, s[72:75], s84 offen offset:2048  lds
QK 220,56,216
C 33,34
KR 16,240,36864
QK 220,60,220
buffer_store_dwordx4 v[0:3], v247, s[76:79], s98 offen offset:0 sc0 sc1
PV 96,64,232
KR 20,240,37888
MU 27,250
PV 96,96,236
MU 15,250
MU 58,251
PV 100,68,232
KR 24,240,38912
C 34,36
PV 100,100,236
E 192
PV 104,72,232
KR 28,240,39936
MU 51,251
PV 104,104,236
buffer_load_dwordx4 v245, s[72:75], s84 offen offset:3072  lds
PV 108,76,232
KR 32,240,40960
MU 45,251
PV 108,108,236
E 194
PV 112,80,232
KR 36,240,41984
C 35,38
PV 112,112,236
E 193
PV 116,84,232
KR 40,240,43008
C 224,192
PV 116,116,236
buffer_load_dword a244, v252, s[56:59], s60 offen
PV 120,88,232
KR 44,240,44032
MU 21,250
PV 120,120,236
E 195
PV 124,92,232
KR 48,240,45056
C 225,194
PV 124,124,236
E 196
SM 228,232
KR 52,240,46080
s_waitcnt lgkmcnt(5)
SM 228,236
E 198
QZ 192,0,160
KR 56,240,47104
C 8,8
QK 192,4,164
E 197
QK 192,8,168
E 199
QK 192,12,172
C 226,196
C 227,198
QZ 196,16,160
v_permlane16_swap_b32 v32, v34
s_waitcnt lgkmcnt(3)
QK 196,20,164
E 200
E 202
QK 196,24,168
E 201
E 203
MU 46,251
QK 196,28,172
C 228,200
C 229,202
KR 60,240,48128
QZ 200,32,160
E 204
QK 200,36,164
E 206
QK 200,40,168
E 205
E 207
QK 200,44,172
C 230,204
C 231,206
s_waitcnt lgkmcnt(0)
MU 52,251
QZ 204,48,160
MU 28,250
v_permlane16_swap_b32 v33, v35
QK 204,52,164
buffer_store_dwordx4 v[32:35], v247, s[76:79], s99 offen offset:0 sc0 sc1
QK 204,56,168
C 9,10
MU 59,251
QK 204,60,172
MU 47,251
MU 22,250
PV 128,64,224
E 208
PV 128,96,228
E 210
PV 132,68,224
E 209
PV 132,100,228
E 211
PV 136,72,224
C 232,208
C 233,210
PV 136,104,228
C 10,12
MU 53,251
PV 140,76,224
E 212
PV 140,108,228
E 214
PV 144,80,224
E 213
PV 144,112,228
E 215
PV 148,84,224
C 234,212
C 235,214
PV 148,116,228
E 216
PV 152,88,224
E 218
PV 152,120,228
E 217
PV 156,92,224
E 219
PV 156,124,228
E 220
SM 232,224
E 222
SM 232,228
E 221
.LSTEP18:
QZ 208,0,176
E 223
QK 208,4,180
C 236,216
C 237,218
QK 208,8,184
C 40,40
C 238,220
QK 208,12,188
C 239,222
MU 29,250
QZ 212,16,176
MU 23,250
MU 60,251
QK 212,20,180
C 11,14
C 41,42
QK 212,24,184
MU 54,251
v_permlane16_swap_b32 v8, v10
QK 212,28,188
E 192
QZ 216,32,176
E 194
QK 216,36,180
E 193
QK 216,40,184
E 195
QK 216,44,188
C 224,192
C 225,194
QZ 220,48,176
C 16,16
C 42,44
QK 220,52,180
E 196
QK 220,56,184
E 198
QK 220,60,188
E 197
PV 160,64,232
E 199
PV 160,96,236
VR 64,242,0
C 226,196
PV 164,68,232
VR 66,242,4096
C 227,198
PV 164,100,236
VR 96,242,8192
VR 98,242,12288
PV 168,72,232
VR 68,243,0
MU 30,250
PV 168,104,236
VR 70,243,4096
MU 61,251
PV 172,76,232
VR 100,243,8192
MU 55,251
PV 172,108,236
VR 102,243,12288
v_permlane16_swap_b32 v9, v11
PV 176,80,232
VR 72,242,256
C 43,46
PV 176,112,236
VR 74,242,4352
C 17,18
PV 180,84,232
VR 104,242,8448
v_permlane16_swap_b32 v40, v42
PV 180,116,236
VR 106,242,12544
C 48,48
PV 184,88,232
VR 76,243,256
MU 31,250
PV 184,120,236
VR 78,243,4352
VR 108,243,8448
PV 188,92,232
VR 110,243,12544
MU 62,251
PV 188,124,236
VR 80,242,512
C 18,20
SM 236,232
VR 82,242,4608
v_permlane16_swap_b32 v41, v43
SM 236,236
VR 112,242,8704
C 49,50
QZ 192,0,192
VR 114,242,12800
C 24,24
QK 192,4,196
VR 84,243,512
C 19,22
QK 192,8,200
VR 86,243,4608
MU 63,251
QK 192,12,204
VR 116,243,8704
C 50,52
QZ 196,16,192
VR 118,243,12800
v_permlane16_swap_b32 v16, v18
QK 196,20,196
E 200
E 202
QK 196,24,200
E 201
E 203
QK 196,28,204
C 228,200
C 229,202
QZ 200,32,192
VR 88,242,768
VR 90,242,4864
QK 200,36,196
E 204
E 206
QK 200,40,200
E 205
E 207
QK 200,44,204
C 230,204
C 231,206
QZ 204,48,192
buffer_store_dwordx4 v[8:11], v247, s[76:79], s98 offen offset:64 sc0 sc1
QK 204,52,196
VR 120,242,8960
s_waitcnt lgkmcnt(3)
QK 204,56,200
VR 122,242,13056
VR 92,243,768
QK 204,60,204
VR 94,243,4864
C 25,26
PV 64,64,224
VR 124,243,8960
C 56,56
PV 64,96,228
VR 126,243,13056
C 51,54
PV 68,68,224
E 208
PV 68,100,228
E 210
PV 72,72,224
E 209
PV 72,104,228
E 211
PV 76,76,224
C 232,208
C 233,210
PV 76,108,228
E 212
PV 80,80,224
E 214
PV 80,112,228
E 213
PV 84,84,224
E 215
PV 84,116,228
s_waitcnt lgkmcnt(1)
s_waitcnt lgkmcnt(0)
PV 88,88,224
E 216
PV 88,120,228
E 218
PV 92,92,224
E 217
PV 92,124,228
E 219
SM 224,224
E 220
SM 224,228
s_waitcnt vmcnt(3)
s_barrier
s_add_u32 s84, s80, 65536
s_add_u32 m0, s80, 131072
QZ 208,0,208
buffer_load_dwordx4 v244, s[72:75], s84 offen offset:0  lds
QK 208,4,212
KR 0,240,49152
C 234,212
QK 208,8,216
E 222
QK 208,12,220
C 235,214
C 236,216
QZ 212,16,208
KR 4,240,50176
C 237,218
QK 212,20,212
E 221
QK 212,24,216
buffer_load_dwordx4 v245, s[72:75], s84 offen offset:1024  lds
QK 212,28,220
KR 8,240,51200
C 238,220
QZ 216,32,208
E 223
QK 216,36,212
KR 12,240,52224
C 239,222
QK 216,40,216
s_mov_b32 s60, .LSTEP20-.LCODE0
v_permlane16_swap_b32 v17, v19
QK 216,44,220
KR 16,240,53248
C 26,28
QZ 220,48,208
buffer_store_dwordx4 v[40:43], v247, s[76:79], s99 offen offset:64 sc0 sc1
QK 220,52,212
KR 20,240,54272
v_permlane16_swap_b32 v48, v50
QK 220,56,216
buffer_load_dwordx4 v244, s[72:75], s84 offen offset:2048  lds
QK 220,60,220
KR 24,240,55296
C 57,58
PV 96,64,232
E 192
PV 96,96,236
KR 28,240,56320
v_permlane16_swap_b32 v49, v51
PV 100,68,232
E 194
PV 100,100,236
KR 32,240,57344
C 27,30
PV 104,72,232
buffer_load_dwordx4 v245, s[72:75], s84 offen offset:3072  lds
PV 104,104,236
KR 36,240,58368
C 58,60
PV 108,76,232
E 193
PV 108,108,236
KR 40,240,59392
C 224,192
PV 112,80,232
E 195
PV 112,112,236
KR 44,240,60416
C 225,194
PV 116,84,232
buffer_load_dword a244, v252, s[56:59], s60 offen
PV 116,116,236
KR 48,240,61440
v_permlane16_swap_b32 v24, v26
PV 120,88,232
E 196
PV 120,120,236
KR 52,240,62464
C 59,62
PV 124,92,232
E 198
PV 124,124,236
KR 56,240,63488
s_waitcnt lgkmcnt(5)
SM 228,232
E 197
SM 228,236
KR 60,240,64512
C 226,196
QZ 192,0,160
E 199
QK 192,4,164
E 200
QK 192,8,168
E 202
QK 192,12,172
C 227,198
v_permlane16_swap_b32 v25, v27
QZ 196,16,160
E 201
QK 196,20,164
E 203
QK 196,24,168
E 204
QK 196,28,172
C 228,200
C 229,202
s_waitcnt lgkmcnt(1)
QZ 200,32,160
E 206
QK 200,36,164
E 205
QK 200,40,168
E 207
QK 200,44,172
C 230,204
C 231,206
s_waitcnt lgkmcnt(0)
QZ 204,48,160
E 208
QK 204,52,164
E 210
QK 204,56,168
E 209
QK 204,60,172
E 211
PV 128,64,224
C 232,208
C 233,210
PV 128,96,228
E 212
PV 132,68,224
E 214
PV 132,100,228
E 213
PV 136,72,224
E 215
PV 136,104,228
C 234,212
C 235,214
PV 140,76,224
E 216
PV 140,108,228
E 218
PV 144,80,224
E 217
PV 144,112,228
E 219
PV 148,84,224
C 236,216
C 237,218
PV 148,116,228
E 220
PV 152,88,224
E 222
PV 152,120,228
E 221
PV 156,92,224
E 223
PV 156,124,228
C 238,220
C 239,222
SM 232,224
E 192
SM 232,228
E 194
.LSTEP19:
QZ 208,0,176
E 193
QK 208,4,180
E 195
QK 208,8,184
C 224,192
C 225,194
QK 208,12,188
E 196
QZ 212,16,176
E 198
QK 212,20,180
E 197
QK 212,24,184
E 199
QK 212,28,188
C 226,196
C 227,198
QZ 216,32,176
E 200
QK 216,36,180
buffer_store_dwordx4 v[16:19], v247, s[76:79], s98 offen offset:128 sc0 sc1
QK 216,40,184
E 202
QK 216,44,188
E 201
QZ 220,48,176
E 203
QK 220,52,180
C 228,200
C 229,202
QK 220,56,184
E 204
QK 220,60,188
E 206
PV 160,64,232
E 205
PV 160,96,236
VR 64,242,16384
VR 66,242,20480
PV 164,68,232
VR 96,242,24576
C 230,204
PV 164,100,236
VR 98,242,28672
v_permlane16_swap_b32 v56, v58
PV 168,72,232
VR 68,243,16384
v_permlane16_swap_b32 v57, v59
PV 168,104,236
VR 70,243,20480
PV 172,76,232
VR 100,243,24576
PV 172,108,236
VR 102,243,28672
PV 176,80,232
VR 72,242,16640
PV 176,112,236
VR 74,242,20736
PV 180,84,232
VR 104,242,24832
PV 180,116,236
VR 106,242,28928
VR 76,243,16640
PV 184,88,232
VR 78,243,20736
PV 184,120,236
VR 108,243,24832
PV 188,92,232
VR 110,243,28928
PV 188,124,236
VR 80,242,16896
SM 236,232
VR 82,242,20992
SM 236,236
VR 112,242,25088
QZ 192,0,192
VR 114,242,29184
QK 192,4,196
VR 84,243,16896
QK 192,8,200
VR 86,243,20992
QK 192,12,204
VR 116,243,25088
VR 118,243,29184
QZ 196,16,192
VR 88,242,17152
QK 196,20,196
VR 90,242,21248
QK 196,24,200
VR 120,242,25344
QK 196,28,204
VR 122,242,29440
QZ 200,32,192
VR 92,243,17152
QK 200,36,196
buffer_store_dwordx4 v[48:51], v247, s[76:79], s99 offen offset:128 sc0 sc1
QK 200,40,200
E 207
QK 200,44,204
C 231,206
VR 94,243,21248
QZ 204,48,192
VR 124,243,25344
s_waitcnt lgkmcnt(10)
QK 204,52,196
VR 126,243,29440
QK 204,56,200
E 208
QK 204,60,204
E 210
PV 64,64,224
E 209
PV 64,96,228
E 211
PV 68,68,224
C 232,208
C 233,210
PV 68,100,228
E 212
PV 72,72,224
E 214
PV 72,104,228
E 213
PV 76,76,224
s_waitcnt lgkmcnt(2)
C 234,212
PV 76,108,228
E 215
PV 80,80,224
E 216
PV 80,112,228
E 218
PV 84,84,224
s_waitcnt lgkmcnt(0)
C 235,214
PV 84,116,228
E 217
PV 88,88,224
E 219
PV 88,120,228
s_add_u32 s84, s80, 81920
s_add_u32 m0, s80, 147456
PV 92,92,224
C 236,216
C 237,218
PV 92,124,228
E 220
SM 224,224
E 222
SM 224,228
s_waitcnt vmcnt(3)
s_barrier
QZ 208,0,208
buffer_load_dwordx4 v244, s[72:75], s84 offen offset:0  lds
QK 208,4,212
KR 0,241,0
s_mov_b32 s60, .LSTEP21-.LCODE0
QK 208,8,216
E 221
QK 208,12,220
KR 4,241,1024
C 238,220
QZ 212,16,208
E 223
QK 212,20,212
KR 8,241,2048
C 239,222
QK 212,24,216
buffer_load_dwordx4 v245, s[72:75], s84 offen offset:1024  lds
QK 212,28,220
KR 12,241,3072
QZ 216,32,208
E 192
QK 216,36,212
KR 16,241,4096
QK 216,40,216
E 194
QK 216,44,220
KR 20,241,5120
QZ 220,48,208
buffer_load_dwordx4 v244, s[72:75], s84 offen offset:2048  lds
QK 220,52,212
KR 24,241,6144
QK 220,56,216
E 193
QK 220,60,220
KR 28,241,7168
C 224,192
PV 96,64,232
E 195
PV 96,96,236
KR 32,241,8192
C 225,194
PV 100,68,232
E 196
PV 100,100,236
buffer_load_dwordx4 v245, s[72:75], s84 offen offset:3072  lds
PV 104,72,232
KR 36,241,9216
PV 104,104,236
E 198
PV 108,76,232
KR 40,241,10240
PV 108,108,236
E 197
PV 112,80,232
KR 44,241,11264
C 226,196
PV 112,112,236
E 199
PV 116,84,232
buffer_load_dword a244, v252, s[56:59], s60 offen
PV 116,116,236
KR 48,241,12288
C 227,198
PV 120,88,232
E 200
PV 120,120,236
KR 52,241,13312
PV 124,92,232
E 202
PV 124,124,236
KR 56,241,14336
s_waitcnt lgkmcnt(5)
SM 228,232
E 201
SM 228,236
KR 60,241,15360
C 228,200
QZ 192,0,160
E 203
QK 192,4,164
E 204
QK 192,8,168
E 206
QK 192,12,172
E 205
QZ 196,16,160
E 207
QK 196,20,164
C 229,202
s_waitcnt lgkmcnt(3)
QK 196,24,168
C 230,204
C 231,206
QK 196,28,172
buffer_store_dwordx4 v[24:27], v247, s[76:79], s98 offen offset:192 sc0 sc1
QZ 200,32,160
E 208
QK 200,36,164
E 210
QK 200,40,168
E 209
QK 200,44,172
s_waitcnt lgkmcnt(0)
C 232,208
QZ 204,48,160
E 211
QK 204,52,164
E 212
QK 204,56,168
E 214
QK 204,60,172
E 213
PV 128,64,224
C 233,210
C 234,212
PV 128,96,228
E 215
PV 132,68,224
E 216
PV 132,100,228
E 218
PV 136,72,224
E 217
PV 136,104,228
C 235,214
C 236,216
PV 140,76,224
E 219
PV 140,108,228
E 220
PV 144,80,224
E 222
PV 144,112,228
E 221
PV 148,84,224
C 237,218
C 238,220
PV 148,116,228
E 223
PV 152,88,224
E 192
PV 152,120,228
E 194
PV 156,92,224
E 193
PV 156,124,228
E 195
SM 232,224
C 239,222
C 224,192
SM 232,228
E 196
.LSTEP20:
QZ 208,0,176
E 198
QK 208,4,180
E 197
QK 208,8,184
C 225,194
C 226,196
QK 208,12,188
E 199
QZ 212,16,176
E 200
QK 212,20,180
E 202
QK 212,24,184
E 201
QK 212,28,188
buffer_store_dwordx4 v[56:59], v247, s[76:79], s99 offen offset:192 sc0 sc1
QZ 216,32,176
C 227,198
C 228,200
QK 216,36,180
E 203
QK 216,40,184
E 204
QK 216,44,188
E 206
QZ 220,48,176
E 205
QK 220,52,180
C 229,202
C 230,204
QK 220,56,184
E 207
QK 220,60,188
E 208
PV 160,64,232
E 210
PV 160,96,236
VR 64,242,0
VR 66,242,4096
PV 164,68,232
VR 96,242,8192
C 231,206
PV 164,100,236
VR 98,242,12288
PV 168,72,232
VR 68,243,0
PV 168,104,236
VR 70,243,4096
PV 172,76,232
VR 100,243,8192
PV 172,108,236
VR 102,243,12288
PV 176,80,232
VR 72,242,256
PV 176,112,236
VR 74,242,4352
PV 180,84,232
VR 104,242,8448
PV 180,116,236
VR 106,242,12544
VR 76,243,256
PV 184,88,232
VR 78,243,4352
PV 184,120,236
VR 108,243,8448
PV 188,92,232
VR 110,243,12544
PV 188,124,236
VR 80,242,512
SM 236,232
VR 82,242,4608
SM 236,236
VR 112,242,8704
QZ 192,0,192
VR 114,242,12800
QK 192,4,196
VR 84,243,512
QK 192,8,200
VR 86,243,4608
QK 192,12,204
VR 116,243,8704
VR 118,243,12800
QZ 196,16,192
VR 88,242,768
QK 196,20,196
VR 90,242,4864
QK 196,24,200
VR 120,242,8960
QK 196,28,204
VR 122,242,13056
QZ 200,32,192
VR 92,243,768
QK 200,36,196
VR 94,243,4864
QK 200,40,200
VR 124,243,8960
QK 200,44,204
VR 126,243,13056
QZ 204,48,192
E 209
QK 204,52,196
s_waitcnt lgkmcnt(11)
C 232,208
QK 204,56,200
E 211
QK 204,60,204
E 212
PV 64,64,224
E 214
PV 64,96,228
E 213
PV 68,68,224
C 233,210
C 234,212
PV 68,100,228
E 215
PV 72,72,224
E 216
PV 72,104,228
E 218
PV 76,76,224
s_waitcnt lgkmcnt(0)
C 235,214
PV 76,108,228
E 217
PV 80,80,224
E 219
PV 80,112,228
C 236,216
C 237,218
PV 84,84,224
E 220
PV 84,116,228
E 222
PV 88,88,224
E 221
PV 88,120,228
s_add_u32 s84, s80, 98304
s_add_u32 m0, s80, 131072
PV 92,92,224
E 223
PV 92,124,228
C 238,220
C 239,222
SM 224,224
E 192
SM 224,228
s_waitcnt vmcnt(3)
s_barrier
QZ 208,0,208
buffer_load_dwordx4 v244, s[72:75], s84 offen offset:0  lds
QK 208,4,212
KR 0,241,16384
s_mov_b32 s60, .LSTEP22-.LCODE0
QK 208,8,216
E 194
QK 208,12,220
KR 4,241,17408
QZ 212,16,208
E 193
QK 212,20,212
KR 8,241,18432
C 224,192
QK 212,24,216
buffer_load_dwordx4 v245, s[72:75], s84 offen offset:1024  lds
QK 212,28,220
KR 12,241,19456
QZ 216,32,208
E 195
QK 216,36,212
KR 16,241,20480
C 225,194
QK 216,40,216
E 196
QK 216,44,220
KR 20,241,21504
QZ 220,48,208
buffer_load_dwordx4 v244, s[72:75], s84 offen offset:2048  lds
QK 220,52,212
KR 24,241,22528
QK 220,56,216
E 198
QK 220,60,220
KR 28,241,23552
PV 96,64,232
E 197
PV 96,96,236
KR 32,241,24576
C 226,196
PV 100,68,232
E 199
PV 100,100,236
buffer_load_dwordx4 v245, s[72:75], s84 offen offset:3072  lds
PV 104,72,232
KR 36,241,25600
C 227,198
PV 104,104,236
E 200
PV 108,76,232
KR 40,241,26624
PV 108,108,236
E 202
PV 112,80,232
KR 44,241,27648
PV 112,112,236
E 201
PV 116,84,232
buffer_load_dword a244, v252, s[56:59], s60 offen
PV 116,116,236
KR 48,241,28672
C 228,200
PV 120,88,232
E 203
PV 120,120,236
KR 52,241,29696
C 229,202
PV 124,92,232
E 204
PV 124,124,236
KR 56,241,30720
s_waitcnt lgkmcnt(5)
SM 228,232
E 206
SM 228,236
KR 60,241,31744
QZ 192,0,160
E 205
QK 192,4,164
E 207
QK 192,8,168
C 230,204
C 231,206
QK 192,12,172
E 208
QZ 196,16,160
E 210
QK 196,20,164
E 209
QK 196,24,168
s_waitcnt lgkmcnt(2)
C 232,208
QK 196,28,172
E 211
QZ 200,32,160
s_waitcnt lgkmcnt(0)
C 233,210
QK 200,36,164
E 212
QK 200,40,168
E 214
QK 200,44,172
E 213
QZ 204,48,160
E 215
QK 204,52,164
C 234,212
C 235,214
QK 204,56,168
E 216
QK 204,60,172
E 218
PV 128,64,224
E 217
PV 128,96,228
E 219
PV 132,68,224
C 236,216
C 237,218
PV 132,100,228
E 220
PV 136,72,224
E 222
PV 136,104,228
E 221
PV 140,76,224
E 223
PV 140,108,228
C 238,220
C 239,222
PV 144,80,224
E 192
PV 144,112,228
E 194
PV 148,84,224
E 193
PV 148,116,228
E 195
PV 152,88,224
E 196
PV 152,120,228
E 198
PV 156,92,224
E 197
PV 156,124,228
E 199
SM 232,224
C 224,192
C 225,194
SM 232,228
C 226,196
C 227,198
.LSTEP21:
QZ 208,0,176
E 200
QK 208,4,180
E 202
QK 208,8,184
E 201
QK 208,12,188
E 203
QZ 212,16,176
C 228,200
C 229,202
QK 212,20,180
E 204
QK 212,24,184
E 206
QK 212,28,188
E 205
QZ 216,32,176
E 207
QK 216,36,180
C 230,204
C 231,206
QK 216,40,184
E 208
QK 216,44,188
E 210
QZ 220,48,176
E 209
QK 220,52,180
E 211
QK 220,56,184
E 212
QK 220,60,188
E 214
PV 160,64,232
E 213
PV 160,96,236
VR 64,242,16384
VR 66,242,20480
PV 164,68,232
VR 96,242,24576
PV 164,100,236
VR 98,242,28672
PV 168,72,232
VR 68,243,16384
PV 168,104,236
VR 70,243,20480
PV 172,76,232
VR 100,243,24576
PV 172,108,236
VR 102,243,28672
PV 176,80,232
VR 72,242,16640
PV 176,112,236
VR 74,242,20736
PV 180,84,232
VR 104,242,24832
PV 180,116,236
VR 106,242,28928
VR 76,243,16640
PV 184,88,232
VR 78,243,20736
PV 184,120,236
VR 108,243,24832
PV 188,92,232
VR 110,243,28928
PV 188,124,236
VR 80,242,16896
SM 236,232
VR 82,242,20992
C 232,208
SM 236,236
VR 112,242,25088
C 233,210
QZ 192,0,192
VR 114,242,29184
C 234,212
QK 192,4,196
VR 84,243,16896
QK 192,8,200
VR 86,243,20992
QK 192,12,204
VR 116,243,25088
VR 118,243,29184
QZ 196,16,192
VR 88,242,17152
QK 196,20,196
VR 90,242,21248
QK 196,24,200
VR 120,242,25344
QK 196,28,204
VR 122,242,29440
QZ 200,32,192
VR 92,243,17152
QK 200,36,196
VR 94,243,21248
QK 200,40,200
VR 124,243,25344
QK 200,44,204
VR 126,243,29440
QZ 204,48,192
E 215
QK 204,52,196
s_waitcnt lgkmcnt(11)
C 235,214
QK 204,56,200
E 216
QK 204,60,204
E 218
PV 64,64,224
E 217
PV 64,96,228
E 219
PV 68,68,224
C 236,216
C 237,218
PV 68,100,228
E 220
PV 72,72,224
E 222
PV 72,104,228
E 221
PV 76,76,224
s_waitcnt lgkmcnt(0)
C 238,220
PV 76,108,228
E 223
PV 80,80,224
E 192
PV 80,112,228
E 194
PV 84,84,224
E 193
PV 84,116,228
E 195
PV 88,88,224
E 196
PV 88,120,228
s_add_u32 s84, s80, 114688
s_add_u32 m0, s80, 147456
PV 92,92,224
E 198
PV 92,124,228
E 197
SM 224,224
s_waitcnt vmcnt(1)
C 239,222
SM 224,228
s_barrier
s_mov_b32 s60, .LSTEP23-.LCODE0
QZ 208,0,208
buffer_load_dwordx4 v244, s[72:75], s84 offen offset:0  lds
QK 208,4,212
KR 0,241,32768
C 224,192
QK 208,8,216
C 225,194
C 226,196
QK 208,12,220
KR 4,241,33792
QZ 212,16,208
E 199
QK 212,20,212
KR 8,241,34816
C 227,198
QK 212,24,216
buffer_load_dwordx4 v245, s[72:75], s84 offen offset:1024  lds
QK 212,28,220
KR 12,241,35840
QZ 216,32,208
E 200
QK 216,36,212
KR 16,241,36864
QK 216,40,216
E 202
QK 216,44,220
KR 20,241,37888
QZ 220,48,208
buffer_load_dwordx4 v244, s[72:75], s84 offen offset:2048  lds
QK 220,52,212
KR 24,241,38912
QK 220,56,216
E 201
QK 220,60,220
KR 28,241,39936
C 228,200
PV 96,64,232
E 203
PV 96,96,236
KR 32,241,40960
C 229,202
PV 100,68,232
E 204
PV 100,100,236
buffer_load_dwordx4 v245, s[72:75], s84 offen offset:3072  lds
PV 104,72,232
KR 36,241,41984
PV 104,104,236
E 206
PV 108,76,232
KR 40,241,43008
PV 108,108,236
E 205
PV 112,80,232
KR 44,241,44032
C 230,204
PV 112,112,236
E 207
PV 116,84,232
buffer_load_dword a244, v252, s[56:59], s60 offen
PV 116,116,236
KR 48,241,45056
C 231,206
PV 120,88,232
E 208
PV 120,120,236
KR 52,241,46080
PV 124,92,232
E 210
PV 124,124,236
KR 56,241,47104
s_waitcnt lgkmcnt(5)
SM 228,232
E 209
SM 228,236
KR 60,241,48128
C 232,208
QZ 192,0,160
E 211
QK 192,4,164
E 212
QK 192,8,168
E 214
QK 192,12,172
E 213
QZ 196,16,160
s_waitcnt lgkmcnt(3)
C 233,210
QK 196,20,164
E 215
QK 196,24,168
C 234,212
C 235,214
QK 196,28,172
E 216
QZ 200,32,160
E 218
QK 200,36,164
E 217
QK 200,40,168
s_waitcnt lgkmcnt(0)
C 236,216
QK 200,44,172
E 219
QZ 204,48,160
E 220
QK 204,52,164
E 222
QK 204,56,168
E 221
QK 204,60,172
C 237,218
C 238,220
PV 128,64,224
E 223
PV 128,96,228
E 192
PV 132,68,224
E 194
PV 132,100,228
E 193
PV 136,72,224
E 195
PV 136,104,228
E 196
PV 140,76,224
E 198
PV 140,108,228
E 197
PV 144,80,224
E 199
PV 144,112,228
E 200
PV 148,84,224
E 202
PV 148,116,228
E 201
PV 152,88,224
E 203
PV 152,120,228
E 204
PV 156,92,224
E 206
PV 156,124,228
E 205
SM 232,224
C 239,222
C 224,192
SM 232,228
C 225,194
C 226,196
.LSTEP22:
QZ 208,0,176
C 227,198
C 228,200
QK 208,4,180
C 229,202
C 230,204
QK 208,8,184
E 207
QK 208,12,188
C 231,206
QZ 212,16,176
QK 212,20,180
QK 212,24,184
QK 212,28,188
QZ 216,32,176
s_nop 1
E 208
QK 216,36,180
E 210
QK 216,40,184
E 209
QK 216,44,188
E 211
QZ 220,48,176
E 212
QK 220,52,180
E 214
QK 220,56,184
E 213
QK 220,60,188
E 215
PV 160,64,232
E 216
PV 160,96,236
VR 64,242,0
VR 66,242,4096
PV 164,68,232
VR 96,242,8192
PV 164,100,236
VR 98,242,12288
PV 168,72,232
VR 68,243,0
PV 168,104,236
VR 70,243,4096
PV 172,76,232
VR 100,243,8192
PV 172,108,236
VR 102,243,12288
PV 176,80,232
VR 72,242,256
PV 176,112,236
VR 74,242,4352
PV 180,84,232
VR 104,242,8448
PV 180,116,236
VR 106,242,12544
VR 76,243,256
PV 184,88,232
VR 78,243,4352
PV 184,120,236
VR 108,243,8448
PV 188,92,232
VR 110,243,12544
PV 188,124,236
VR 80,242,512
SM 236,232
VR 82,242,4608
C 232,208
SM 236,236
VR 112,242,8704
C 233,210
QZ 192,0,192
VR 114,242,12800
C 234,212
QK 192,4,196
VR 84,243,512
C 235,214
QK 192,8,200
VR 86,243,4608
QK 192,12,204
VR 116,243,8704
VR 118,243,12800
QZ 196,16,192
VR 88,242,768
QK 196,20,196
VR 90,242,4864
QK 196,24,200
VR 120,242,8960
QK 196,28,204
VR 122,242,13056
QZ 200,32,192
VR 92,243,768
QK 200,36,196
VR 94,243,4864
QK 200,40,200
VR 124,243,8960
QK 200,44,204
VR 126,243,13056
QZ 204,48,192
E 218
QK 204,52,196
E 217
QK 204,56,200
s_waitcnt lgkmcnt(10)
C 236,216
QK 204,60,204
E 219
PV 64,64,224
E 220
PV 64,96,228
E 222
PV 68,68,224
E 221
PV 68,100,228
C 237,218
C 238,220
PV 72,72,224
E 223
PV 72,104,228
E 192
PV 76,76,224
s_waitcnt lgkmcnt(0)
C 239,222
PV 76,108,228
E 194
PV 80,80,224
E 193
PV 80,112,228
E 195
PV 84,84,224
E 196
PV 84,116,228
E 198
PV 88,88,224
E 197
PV 88,120,228
E 199
PV 92,92,224
E 200
PV 92,124,228
E 202
SM 224,224
s_waitcnt vmcnt(1)
C 224,192
SM 224,228
s_barrier
C 225,194
QZ 208,0,208
C 226,196
C 227,198
QK 208,4,212
KR 0,241,49152
QK 208,8,216
E 201
QK 208,12,220
KR 4,241,50176
C 228,200
QZ 212,16,208
E 203
QK 212,20,212
KR 8,241,51200
C 229,202
QK 212,24,216
E 204
QK 212,28,220
KR 12,241,52224
QZ 216,32,208
E 206
QK 216,36,212
KR 16,241,53248
QK 216,40,216
E 205
QK 216,44,220
KR 20,241,54272
C 230,204
QZ 220,48,208
E 207
QK 220,52,212
KR 24,241,55296
C 231,206
QK 220,56,216
E 208
QK 220,60,220
KR 28,241,56320
PV 96,64,232
E 210
PV 96,96,236
KR 32,241,57344
PV 100,68,232
E 209
PV 100,100,236
KR 36,241,58368
PV 104,72,232
E 211
PV 104,104,236
KR 40,241,59392
PV 108,76,232
E 212
PV 108,108,236
KR 44,241,60416
PV 112,80,232
E 214
PV 112,112,236
KR 48,241,61440
PV 116,84,232
E 213
PV 116,116,236
KR 52,241,62464
PV 120,88,232
E 215
PV 120,120,236
KR 56,241,63488
PV 124,92,232
E 216
PV 124,124,236
KR 60,241,64512
s_waitcnt lgkmcnt(6)
SM 228,232
C 232,208
C 233,210
SM 228,236
C 234,212
C 235,214
QZ 192,0,160
E 218
QK 192,4,164
E 217
QK 192,8,168
E 219
QK 192,12,172
C 236,216
C 237,218
QZ 196,16,160
E 220
QK 196,20,164
E 222
QK 196,24,168
E 221
QK 196,28,172
s_waitcnt lgkmcnt(0)
C 238,220
QZ 200,32,160
E 223
QK 200,36,164
E 192
QK 200,40,168
E 194
QK 200,44,172
E 193
QZ 204,48,160
E 195
QK 204,52,164
E 196
QK 204,56,168
E 198
QK 204,60,172
E 197
PV 128,64,224
E 199
PV 128,96,228
E 200
PV 132,68,224
E 202
PV 132,100,228
E 201
PV 136,72,224
E 203
PV 136,104,228
E 204
PV 140,76,224
E 206
PV 140,108,228
E 205
PV 144,80,224
E 207
PV 144,112,228
C 239,222
PV 148,84,224
PV 148,116,228
PV 152,88,224
PV 152,120,228
PV 156,92,224
PV 156,124,228
SM 232,224
C 224,192
C 225,194
SM 232,228
C 226,196
C 227,198
.LSTEP23:
QZ 208,0,176
C 228,200
C 229,202
QK 208,4,180
C 230,204
C 231,206
QK 208,8,184
QK 208,12,188
QZ 212,16,176
QK 212,20,180
QK 212,24,184
QK 212,28,188
QZ 216,32,176
s_nop 2
E 208
QK 216,36,180
E 210
QK 216,40,184
E 209
QK 216,44,188
E 211
QZ 220,48,176
E 212
QK 220,52,180
E 214
QK 220,56,184
E 213
QK 220,60,188
E 215
PV 160,64,232
E 216
PV 160,96,236
VR 64,242,16384
VR 66,242,20480
PV 164,68,232
VR 96,242,24576
PV 164,100,236
VR 98,242,28672
PV 168,72,232
VR 68,243,16384
PV 168,104,236
VR 70,243,20480
PV 172,76,232
VR 100,243,24576
PV 172,108,236
VR 102,243,28672
PV 176,80,232
VR 72,242,16640
PV 176,112,236
VR 74,242,20736
PV 180,84,232
VR 104,242,24832
PV 180,116,236
VR 106,242,28928
VR 76,243,16640
PV 184,88,232
VR 78,243,20736
PV 184,120,236
VR 108,243,24832
PV 188,92,232
VR 110,243,28928
PV 188,124,236
VR 80,242,16896
SM 236,232
VR 82,242,20992
C 232,208
SM 236,236
VR 112,242,25088
C 233,210
QZ 192,0,192
VR 114,242,29184
C 234,212
QK 192,4,196
VR 84,243,16896
C 235,214
QK 192,8,200
VR 86,243,20992
QK 192,12,204
VR 116,243,25088
VR 118,243,29184
QZ 196,16,192
VR 88,242,17152
QK 196,20,196
VR 90,242,21248
QK 196,24,200
VR 120,242,25344
QK 196,28,204
VR 122,242,29440
QZ 200,32,192
VR 92,243,17152
QK 200,36,196
VR 94,243,21248
QK 200,40,200
VR 124,243,25344
QK 200,44,204
VR 126,243,29440
QZ 204,48,192
E 218
QK 204,52,196
E 217
QK 204,56,200
s_waitcnt lgkmcnt(10)
C 236,216
QK 204,60,204
E 219
PV 64,64,224
E 220
PV 64,96,228
E 222
PV 68,68,224
E 221
PV 68,100,228
C 237,218
C 238,220
PV 72,72,224
E 223
PV 72,104,228
E 192
PV 76,76,224
s_waitcnt lgkmcnt(0)
C 239,222
PV 76,108,228
E 194
PV 80,80,224
E 196
PV 80,112,228
E 198
PV 84,84,224
E 193
PV 84,116,228
E 195
PV 88,88,224
E 197
PV 88,120,228
E 199
PV 92,92,224
E 200
PV 92,124,228
E 202
SM 224,224
E 204
SM 224,228
E 206
QZ 208,0,208
E 201
QK 208,4,212
E 203
QK 208,8,216
E 205
QK 208,12,220
v_accvgpr_read_b32 v248, a224
v_subrev_u32 v253, s88, v248
QZ 212,16,208
v_rcp_f32 v248, v248
s_nop 0
MU 64,248
QK 212,20,212
MU 72,248
v_max_u32 v254, v254, v253
QK 212,24,216
MU 65,248
MU 80,248
MU 88,248
QK 212,28,220
MU 73,248
s_add_u32 s96, s81, 131072
QZ 216,32,208
MU 66,248
C 224,192
QK 216,36,212
MU 67,248
MU 81,248
QK 216,40,216
MU 74,248
MU 89,248
QK 216,44,220
MU 68,248
C 225,194
QZ 220,48,208
MU 75,248
MU 82,248
QK 220,52,212
MU 69,248
C 226,196
QK 220,56,216
MU 90,248
C 227,198
QK 220,60,220
MU 70,248
MU 76,248
MU 83,248
PV 96,64,232
MU 71,248
C 228,200
PV 96,96,236
MU 77,248
C 229,202
PV 100,68,232
C 64,64
MU 84,248
MU 91,248
PV 100,100,236
C 230,204
C 65,66
PV 104,72,232
MU 78,248
C 66,68
PV 104,104,236
MU 85,248
MU 79,248
PV 108,76,232
MU 92,248
C 67,70
PV 108,108,236
E 207
PV 112,80,232
C 72,72
C 231,206
PV 112,112,236
v_permlane16_swap_b32 v64, v66
MU 86,248
PV 116,84,232
MU 93,248
v_permlane16_swap_b32 v65, v67
PV 116,116,236
C 73,74
MU 87,248
PV 120,88,232
buffer_store_dwordx4 v[64:67], v247, s[76:79], s96 offen offset:0 sc0 sc1
PV 120,120,236
C 74,76
MU 94,248
PV 124,92,232
C 80,80
C 75,78
PV 124,124,236
MU 95,248
v_permlane16_swap_b32 v72, v74
SM 228,232
C 81,82
v_permlane16_swap_b32 v73, v75
SM 228,236
C 82,84
C 88,88
PV 128,64,224
C 83,86
C 89,90
PV 128,96,228
v_permlane16_swap_b32 v80, v82
C 90,92
PV 132,68,224
E 208
PV 132,100,228
buffer_store_dwordx4 v[72:75], v247, s[76:79], s96 offen offset:64 sc0 sc1
PV 136,72,224
v_accvgpr_read_b32 v249, a228
v_subrev_u32 v253, s88, v249
v_rcp_f32 v249, v249
s_nop 0
MU 96,249
PV 136,104,228
MU 104,249
v_max_u32 v254, v254, v253
PV 140,76,224
MU 97,249
MU 112,249
MU 120,249
PV 140,108,228
MU 105,249
s_add_u32 s97, s81, 135168
PV 144,80,224
MU 98,249
MU 99,249
PV 144,112,228
MU 113,249
MU 106,249
PV 148,84,224
MU 121,249
MU 100,249
PV 148,116,228
E 210
PV 152,88,224
MU 107,249
MU 114,249
PV 152,120,228
v_permlane16_swap_b32 v81, v83
MU 101,249
PV 156,92,224
E 212
E 214
MU 122,249
PV 156,124,228
E 209
E 211
E 213
E 215
E 216
E 218
E 220
E 222
buffer_store_dwordx4 v[80:83], v247, s[76:79], s96 offen offset:128 sc0 sc1
C 91,94
MU 102,249
MU 108,249
MU 115,249
SM 232,224
C 232,208
C 233,210
C 234,212
C 235,214
E 217
E 219
E 221
E 223
MU 103,249
SM 232,228
C 236,216
C 237,218
C 238,220
C 239,222
MU 109,249
PV 160,64,232
C 96,96
MU 116,249
MU 123,249
PV 160,96,236
v_permlane16_swap_b32 v88, v90
C 97,98
PV 164,68,232
MU 110,249
C 98,100
PV 164,100,236
MU 117,249
v_accvgpr_read_b32 v250, a232
PV 168,72,232
MU 111,249
MU 124,249
v_subrev_u32 v253, s88, v250
v_rcp_f32 v250, v250
s_nop 0
MU 128,250
PV 168,104,236
v_permlane16_swap_b32 v89, v91
C 99,102
MU 136,250
PV 172,76,232
C 104,104
MU 129,250
MU 144,250
MU 152,250
PV 172,108,236
buffer_store_dwordx4 v[88:91], v247, s[76:79], s96 offen offset:192 sc0 sc1
v_permlane16_swap_b32 v96, v98
MU 118,249
MU 137,250
PV 176,80,232
MU 125,249
MU 130,250
PV 176,112,236
v_permlane16_swap_b32 v97, v99
C 105,106
MU 131,250
MU 145,250
PV 180,84,232
MU 119,249
MU 138,250
MU 153,250
PV 180,116,236
buffer_store_dwordx4 v[96:99], v247, s[76:79], s97 offen offset:0 sc0 sc1
C 106,108
MU 132,250
PV 184,88,232
MU 126,249
MU 139,250
MU 146,250
PV 184,120,236
C 112,112
MU 133,250
PV 188,92,232
C 107,110
MU 154,250
PV 188,124,236
MU 127,249
MU 134,250
MU 140,250
MU 147,250
SM 236,232
v_permlane16_swap_b32 v104, v106
C 113,114
MU 135,250
SM 236,236
v_max_u32 v254, v254, v253
MU 141,250
C 128,128
MU 148,250
MU 155,250
v_permlane16_swap_b32 v105, v107
C 114,116
C 120,120
s_add_u32 s98, s81, 139264
C 129,130
MU 142,250
buffer_store_dwordx4 v[104:107], v247, s[76:79], s97 offen offset:64 sc0 sc1
C 130,132
MU 149,250
C 115,118
MU 143,250
MU 156,250
v_accvgpr_read_b32 v251, a236
v_subrev_u32 v253, s88, v251
v_rcp_f32 v251, v251
C 121,122
MU 160,251
C 131,134
MU 168,251
C 136,136
MU 161,251
MU 176,251
MU 184,251
v_permlane16_swap_b32 v112, v114
v_permlane16_swap_b32 v128, v130
MU 150,250
MU 169,251
MU 157,250
MU 162,251
C 122,124
v_permlane16_swap_b32 v129, v131
C 137,138
MU 163,251
MU 177,251
MU 151,250
MU 170,251
MU 185,251
v_permlane16_swap_b32 v113, v115
buffer_store_dwordx4 v[128:131], v247, s[76:79], s98 offen offset:0 sc0 sc1
C 138,140
MU 164,251
MU 158,250
MU 171,251
MU 178,251
buffer_store_dwordx4 v[112:115], v247, s[76:79], s97 offen offset:128 sc0 sc1
C 123,126
C 144,144
MU 165,251
C 139,142
MU 186,251
MU 159,250
MU 166,251
MU 172,251
MU 179,251
v_permlane16_swap_b32 v136, v138
C 145,146
MU 167,251
v_permlane16_swap_b32 v120, v122
v_max_u32 v254, v254, v253
MU 173,251
C 160,160
MU 180,251
MU 187,251
v_permlane16_swap_b32 v137, v139
C 146,148
C 152,152
s_add_u32 s99, s81, 143360
C 161,162
MU 174,251
v_permlane16_swap_b32 v121, v123
buffer_store_dwordx4 v[136:139], v247, s[76:79], s98 offen offset:64 sc0 sc1
C 162,164
MU 181,251
C 147,150
MU 175,251
MU 188,251
buffer_store_dwordx4 v[120:123], v247, s[76:79], s97 offen offset:192 sc0 sc1
C 153,154
C 163,166
C 168,168
v_permlane16_swap_b32 v144, v146
v_permlane16_swap_b32 v160, v162
MU 182,251
MU 189,251
C 154,156
v_permlane16_swap_b32 v161, v163
C 169,170
MU 183,251
v_permlane16_swap_b32 v145, v147
buffer_store_dwordx4 v[160:163], v247, s[76:79], s99 offen offset:0 sc0 sc1
C 170,172
MU 190,251
buffer_store_dwordx4 v[144:147], v247, s[76:79], s98 offen offset:128 sc0 sc1
C 155,158
C 176,176
C 171,174
MU 191,251
v_permlane16_swap_b32 v168, v170
C 177,178
v_permlane16_swap_b32 v152, v154
v_permlane16_swap_b32 v169, v171
C 178,180
C 184,184
v_permlane16_swap_b32 v153, v155
buffer_store_dwordx4 v[168:171], v247, s[76:79], s99 offen offset:64 sc0 sc1
C 179,182
buffer_store_dwordx4 v[152:155], v247, s[76:79], s98 offen offset:192 sc0 sc1
C 185,186
v_permlane16_swap_b32 v176, v178
C 186,188
v_permlane16_swap_b32 v177, v179
buffer_store_dwordx4 v[176:179], v247, s[76:79], s99 offen offset:128 sc0 sc1
C 187,190
v_permlane16_swap_b32 v184, v186
s_nop 0
v_permlane16_swap_b32 v185, v187
buffer_store_dwordx4 v[184:187], v247, s[76:79], s99 offen offset:192 sc0 sc1
s_mov_b32 s92, 0x3c000000
v_cmp_lt_u32 vcc, s92, v254
s_nop 4
s_cmp_lg_u64 vcc, 0
s_cselect_b32 %[bad], 1, 0
.LCODEEND:
.purgem QZ
.purgem QK
.purgem PZ
.purgem PV
.purgem SZ
.purgem SM
.purgem SF
.purgem E
.purgem C
.purgem KR
.purgem VR
.purgem MU
)ASM"
      : [bad] "=s"(bad)
      : [qlo] "s"((unsigned)qa), [qhi] "s"((unsigned)(qa >> 32)), [klo] "s"((unsigned)ka), [khi] "s"((unsigned)(ka >> 32)),
        [vlo] "s"((unsigned)va), [vhi] "s"((unsigned)(va >> 32)), [olo] "s"((unsigned)oa), [ohi] "s"((unsigned)(oa >> 32)),
        [wv] "s"(wv)
      : "memory", "vcc", "scc", "m0", "s56","s57","s58","s59","s60","s61","s62","s63","s64","s65","s66","s67","s68","s69","s70","s71","s72","s73","s74","s75","s76","s77","s78","s79","s80","s81","s82","s83","s84","s85","s86","s87","s88","s89","s90","s91","s92","s93","s94","s95","s96","s97","s98","s99", "v0","v1","v2","v3","v4","v5","v6","v7","v8","v9","v10","v11","v12","v13","v14","v15","v16","v17","v18","v19","v20","v21","v22","v23","v24","v25","v26","v27","v28","v29","v30","v31","v32","v33","v34","v35","v36","v37","v38","v39","v40","v41","v42","v43","v44","v45","v46","v47","v48","v49","v50","v51","v52","v53","v54","v55","v56","v57","v58","v59","v60","v61","v62","v63","v64","v65","v66","v67","v68","v69","v70","v71","v72","v73","v74","v75","v76","v77","v78","v79","v80","v81","v82","v83","v84","v85","v86","v87","v88","v89","v90","v91","v92","v93","v94","v95","v96","v97","v98","v99","v100","v101","v102","v103","v104","v105","v106","v107","v108","v109","v110","v111","v112","v113","v114","v115","v116","v117","v118","v119","v120","v121","v122","v123","v124","v125","v126","v127","v128","v129","v130","v131","v132","v133","v134","v135","v136","v137","v138","v139","v140","v141","v142","v143","v144","v145","v146","v147","v148","v149","v150","v151","v152","v153","v154","v155","v156","v157","v158","v159","v160","v161","v162","v163","v164","v165","v166","v167","v168","v169","v170","v171","v172","v173","v174","v175","v176","v177","v178","v179","v180","v181","v182","v183","v184","v185","v186","v187","v188","v189","v190","v191","v192","v193","v194","v195","v196","v197","v198","v199","v200","v201","v202","v203","v204","v205","v206","v207","v208","v209","v210","v211","v212","v213","v214","v215","v216","v217","v218","v219","v220","v221","v222","v223","v224","v225","v226","v227","v228","v229","v230","v231","v232","v233","v234","v235","v236","v237","v238","v239","v240","v241","v242","v243","v244","v245","v246","v247","v248","v249","v250","v251","v252","v253","v254","v255", "a0","a1","a2","a3","a4","a5","a6","a7","a8","a9","a10","a11","a12","a13","a14","a15","a16","a17","a18","a19","a20","a21","a22","a23","a24","a25","a26","a27","a28","a29","a30","a31","a32","a33","a34","a35","a36","a37","a38","a39","a40","a41","a42","a43","a44","a45","a46","a47","a48","a49","a50","a51","a52","a53","a54","a55","a56","a57","a58","a59","a60","a61","a62","a63","a64","a65","a66","a67","a68","a69","a70","a71","a72","a73","a74","a75","a76","a77","a78","a79","a80","a81","a82","a83","a84","a85","a86","a87","a88","a89","a90","a91","a92","a93","a94","a95","a96","a97","a98","a99","a100","a101","a102","a103","a104","a105","a106","a107","a108","a109","a110","a111","a112","a113","a114","a115","a116","a117","a118","a119","a120","a121","a122","a123","a124","a125","a126","a127","a128","a129","a130","a131","a132","a133","a134","a135","a136","a137","a138","a139","a140","a141","a142","a143","a144","a145","a146","a147","a148","a149","a150","a151","a152","a153","a154","a155","a156","a157","a158","a159","a160","a161","a162","a163","a164","a165","a166","a167","a168","a169","a170","a171","a172","a173","a174","a175","a176","a177","a178","a179","a180","a181","a182","a183","a184","a185","a186","a187","a188","a189","a190","a191","a192","a193","a194","a195","a196","a197","a198","a199","a200","a201","a202","a203","a204","a205","a206","a207","a208","a209","a210","a211","a212","a213","a214","a215","a216","a217","a218","a219","a220","a221","a222","a223","a224","a225","a226","a227","a228","a229","a230","a231","a232","a233","a234","a235","a236","a237","a238","a239","a240","a241","a242","a243","a244","a245","a246","a247","a248","a249","a250","a251","a252","a253","a254","a255");
  (void)grp;
  if (bad) {
    asm volatile("s_waitcnt vmcnt(0)" ::: "memory");
    slow_rows(Qb, Kb, Vb, Ob, wv);
  }
}

extern "C" void kernel_launch(void* const* d_in, const int* in_sizes, int n_in, void* d_out, int out_size, void* d_ws, size_t ws_size,
                              hipStream_t stream) {
  (void)in_sizes; (void)n_in; (void)out_size; (void)ws_size;
  (void)hipFuncSetAttribute((const void*)gqa_fwd, hipFuncAttributeMaxDynamicSharedMemorySize, 163840);
  (void)d_ws;
  hipLaunchKernelGGL(gqa_fwd, dim3(256), dim3(256), 163840, stream, (const char*)d_in[0], (const char*)d_in[1], (const char*)d_in[2], (char*)d_out);
}
```
